```python
import torch

def _softmax(x, axis=-1):
    return torch.softmax(x, dim=axis)
def _gelu(x):
    return torch.nn.functional.gelu(x, approximate="tanh")
def _silu(x):
    return torch.nn.functional.silu(x)
def _sigmoid(x):
    return torch.sigmoid(x)
def _layernorm(x, axis=-1, eps=1e-5):
    m = x.mean(dim=axis, keepdim=True)
    v = x.var(dim=axis, keepdim=True, unbiased=False)
    return (x - m) / torch.sqrt(v + eps)
def _scatter_add(tgt, idx, src, axis=0):
    return tgt.clone().index_add_(axis, idx, src)
def _segment_sum(src, seg, n):
    out = torch.zeros((n,) + src.shape[1:], dtype=src.dtype)
    return out.index_add_(0, seg, src)
def _pad(x, pw):
    flat = tuple(v for pair in reversed(pw) for v in pair)
    return torch.nn.functional.pad(x, flat)

kernel_name = 'parallel_sum__linear_kernel_hedgehog__dense_activation_relu__prompt'

def setup_inputs():

    _w = (lambda a: (lambda t: t.to(torch.bfloat16) if t.is_floating_point() else t)(torch.as_tensor(a)))
    def _A_setup():
        B, S, D, H = 1, 2048, 576, 32
        Dh = D // H
        return {
            "x": _w((lambda *s: torch.randn(*s, dtype=torch.bfloat16) * 0.05)(B, S, D)),
            "wqkv": _w((lambda *s: torch.randn(*s, dtype=torch.bfloat16) * 0.8)(D, 3 * D)),
            "w_phi": _w((lambda *s: torch.randn(*s, dtype=torch.bfloat16) * 0.8)(H, Dh, Dh) * (Dh ** -0.5)),
            "wo": _w((lambda *s: torch.randn(*s, dtype=torch.bfloat16) * 0.2)(D, D)),
        }
    _A_in = _A_setup()

    def _B_setup():
        B, S, D, Dff = 1, 2048, 576, 4608
        return {
            "x": _w((lambda *s: torch.randn(*s, dtype=torch.bfloat16) * 0.05)(B, S, D)),
            "w1": _w((lambda *s: torch.randn(*s, dtype=torch.bfloat16) * 0.05)(D, Dff)),
            "w2": _w((lambda *s: torch.randn(*s, dtype=torch.bfloat16) * 0.05)(Dff, D)),
        }
    _B_in = _B_setup()

    return {
        "A_x": _A_in["x"],
        "A_wqkv": _A_in["wqkv"],
        "A_w_phi": _A_in["w_phi"],
        "A_wo": _A_in["wo"],
        "B_x": _B_in["x"],
        "B_w1": _B_in["w1"],
        "B_w2": _B_in["w2"],
    }

def reference(A_x, A_wqkv, A_w_phi, A_wo, B_x, B_w1, B_w2):
    def _A_ref(x, wqkv, w_phi, wo):
        B, S, D = x.shape
        H = 32; Dh = D // H
        qkv = (lambda x, *d: x.permute(*d))((x @ wqkv).reshape(B, S, 3, H, Dh), 2, 0, 3, 1, 4)
        q, k, v = qkv[0], qkv[1], qkv[2]
        pq = torch.einsum("bhsd,hdf->bhsf", q, w_phi)
        pk = torch.einsum("bhsd,hdf->bhsf", k, w_phi)
        phi_q = torch.concatenate([_softmax(pq, axis=-1), _softmax(-pq, axis=-1)], axis=-1)
        phi_k = torch.concatenate([_softmax(pk, axis=-1), _softmax(-pk, axis=-1)], axis=-1)
        kv = torch.einsum("bhsf,bhsd->bhfd", phi_k, v)
        z  = torch.einsum("bhsf->bhf", phi_k)
        num = torch.einsum("bhqf,bhfd->bhqd", phi_q, kv)
        den = torch.einsum("bhqf,bhf->bhq", phi_q, z)[..., None] + 1e-6
        o = num / den
        return (lambda x, *d: x.permute(*d))(o, 0, 2, 1, 3).reshape(B, S, D) @ wo

    def _B_ref(x, w1, w2):
        h = torch.einsum("bsd,df->bsf", x, w1)
        zero = torch.zeros_like(h)
        h = torch.maximum(h, zero)
        return torch.einsum("bsf,fd->bsd", h, w2)

    x = A_x
    xn = _layernorm(x, axis=-1)
    return x + _A_ref(xn, A_wqkv, A_w_phi, A_wo) + _B_ref(xn, B_w1, B_w2)
```

```cpp
#include <hip/hip_runtime.h>
#include <hip/hip_bf16.h>
typedef __hip_bfloat16 bf16;
#define S_ 2048
#define D_ 576
#define H_ 32
#define DH 18
#define FF 4608
__device__ __forceinline__ float b2f(bf16 v){return __bfloat162float(v);}
__device__ __forceinline__ bf16 f2b(float v){return __float2bfloat16(v);}
__device__ __forceinline__ float rb(float v){return b2f(f2b(v));}

__global__ void k_ln(const bf16* x, bf16* xn){
  int row=blockIdx.x; int t=threadIdx.x;
  float v[9]; float s=0;
  for(int i=0;i<9;i++){v[i]=b2f(x[row*D_+t+64*i]); s+=v[i];}
  for(int o=32;o;o>>=1) s+=__shfl_xor(s,o);
  float m=s/D_; float q=0;
  for(int i=0;i<9;i++){float d=v[i]-m; q+=d*d;}
  for(int o=32;o;o>>=1) q+=__shfl_xor(q,o);
  float var=q/D_;
  float mb=rb(m), vb=rb(var);
  float sd=rb(sqrtf(rb(vb+1e-5f)));
  for(int i=0;i<9;i++){ xn[row*D_+t+64*i]=f2b(rb(v[i]-mb)/sd); }
}
__global__ void k_gemm(const bf16* A,const bf16* B,bf16* C,int M,int N,int K,int relu){
  __shared__ float As[16][17], Bs[16][17];
  int tx=threadIdx.x, ty=threadIdx.y;
  int col=blockIdx.x*16+tx, row=blockIdx.y*16+ty;
  float acc=0;
  for(int k0=0;k0<K;k0+=16){
    As[ty][tx]=b2f(A[(size_t)row*K+k0+tx]);
    Bs[ty][tx]=b2f(B[(size_t)(k0+ty)*N+col]);
    __syncthreads();
    for(int k=0;k<16;k++) acc+=As[ty][k]*Bs[k][tx];
    __syncthreads();
  }
  if(relu) acc=fmaxf(acc,0.f);
  C[(size_t)row*N+col]=f2b(acc);
}
__global__ __launch_bounds__(256) void k_phi(const bf16* qkv,const bf16* wphi,bf16* phi){
  int idx=blockIdx.x*blockDim.x+threadIdx.x; if(idx>=2*S_*H_) return;
  int h=idx%H_; int s=(idx/H_)%S_; int w=idx/(H_*S_);
  float q[DH]; for(int d=0;d<DH;d++) q[d]=b2f(qkv[(size_t)s*3*D_+w*D_+h*DH+d]);
  float p[DH];
  #pragma unroll
  for(int f=0;f<DH;f++){float a=0;
    #pragma unroll
    for(int d=0;d<DH;d++) a+=q[d]*b2f(wphi[(h*DH+d)*DH+f]); p[f]=rb(a);}
  float mx=-1e30f,mn=1e30f;
  #pragma unroll
  for(int f=0;f<DH;f++){mx=fmaxf(mx,p[f]); mn=fminf(mn,p[f]);}
  float s1=0,s2=0;
  #pragma unroll
  for(int f=0;f<DH;f++){s1+=__expf(p[f]-mx); s2+=__expf(mn-p[f]);}
  bf16* o=phi+(((size_t)w*S_+s)*H_+h)*36;
  #pragma unroll
  for(int f=0;f<DH;f++){o[f]=f2b(__expf(p[f]-mx)/s1); o[18+f]=f2b(__expf(mn-p[f])/s2);}
}
__global__ void k_kv(const bf16* qkv,const bf16* phi,float* kv){
  int h=blockIdx.x; int t=threadIdx.x; if(t>=36*19) return;
  int f=t/19,d=t%19; float a=0;
  const bf16* pk=phi+(size_t)S_*H_*36;
  for(int s=0;s<S_;s++){ float p=b2f(pk[((size_t)s*H_+h)*36+f]); float v=d<18?b2f(qkv[(size_t)s*3*D_+2*D_+h*DH+d]):1.f; a+=p*v;}
  kv[h*36*19+t]=rb(a);
}
__global__ void k_o(const bf16* phi,const float* kv,bf16* o){
  int idx=blockIdx.x*blockDim.x+threadIdx.x; if(idx>=S_*H_) return;
  int h=idx%H_; int s=idx/H_;
  float den=0; const bf16* pq=phi+((size_t)s*H_+h)*36;
  for(int f=0;f<36;f++) den+=b2f(pq[f])*kv[(h*36+f)*19+18];
  den=rb(rb(den)+1e-6f);
  for(int d=0;d<18;d++){ float n=0; for(int f=0;f<36;f++) n+=b2f(pq[f])*kv[(h*36+f)*19+d]; o[(size_t)s*D_+h*DH+d]=f2b(rb(n)/den);}
}
__global__ void k_fin(const bf16* x,const bf16* a,const bf16* b,bf16* out){
  int i=blockIdx.x*blockDim.x+threadIdx.x; if(i>=S_*D_) return;
  out[i]=f2b(rb(b2f(x[i])+b2f(a[i]))+b2f(b[i]));
}
extern "C" void kernel_launch(void* const* d_in, const int* in_sizes, int n_in, void* d_out, int out_size, void* d_ws, size_t ws_size, hipStream_t stream){
  const bf16* x=(const bf16*)d_in[0]; const bf16* wqkv=(const bf16*)d_in[1]; const bf16* wphi=(const bf16*)d_in[2];
  const bf16* wo=(const bf16*)d_in[3]; const bf16* w1=(const bf16*)d_in[5]; const bf16* w2=(const bf16*)d_in[6];
  char* ws=(char*)d_ws;
  bf16* xn=(bf16*)ws; ws+=S_*D_*2;
  bf16* qkv=(bf16*)ws; ws+=S_*3*D_*2;
  bf16* hh=(bf16*)ws; ws+=(size_t)S_*FF*2;
  bf16* phi=(bf16*)ws; ws+=(size_t)2*S_*H_*36*2;
  float* kv=(float*)ws; ws+=H_*36*19*4+256;
  bf16* o=(bf16*)ws; ws+=S_*D_*2;
  bf16* oa=(bf16*)ws; ws+=S_*D_*2;
  bf16* ob=(bf16*)ws; ws+=S_*D_*2;
  k_ln<<<S_,64,0,stream>>>(x,xn);
  k_gemm<<<dim3(3*D_/16,S_/16),dim3(16,16),0,stream>>>(xn,wqkv,qkv,S_,3*D_,D_,0);
  k_gemm<<<dim3(FF/16,S_/16),dim3(16,16),0,stream>>>(xn,w1,hh,S_,FF,D_,1);
  k_phi<<<2*S_*H_/256,256,0,stream>>>(qkv,wphi,phi);
  k_kv<<<H_,704,0,stream>>>(qkv,phi,kv);
  k_o<<<S_*H_/256,256,0,stream>>>(phi,kv,o);
  k_gemm<<<dim3(D_/16,S_/16),dim3(16,16),0,stream>>>(o,wo,oa,S_,D_,D_,0);
  k_gemm<<<dim3(D_/16,S_/16),dim3(16,16),0,stream>>>(hh,w2,ob,S_,D_,FF,0);
  k_fin<<<S_*D_/256,256,0,stream>>>(x,oa,ob,(bf16*)d_out);
}
```

```cpp
#include <hip/hip_runtime.h>
#include <hip/hip_bf16.h>
#include <utility>
typedef __bf16 bf16x8 __attribute__((ext_vector_type(8)));
typedef __bf16 bf16x4 __attribute__((ext_vector_type(4)));
typedef __bf16 bf16x2 __attribute__((ext_vector_type(2)));
typedef short s16x2 __attribute__((ext_vector_type(2)));
typedef float f32x2 __attribute__((ext_vector_type(2)));
typedef float f32x4 __attribute__((ext_vector_type(4)));
typedef unsigned u32x4 __attribute__((ext_vector_type(4)));
typedef unsigned u32x2 __attribute__((ext_vector_type(2)));
typedef unsigned long long u64;
#define DI __device__ __forceinline__
#define LDSP(T, a) ((__attribute__((address_space(3))) T*)(size_t)(a))
#define IC(v) std::integral_constant<int, (v)>{}
#define SB() __builtin_amdgcn_sched_barrier(0)
template <int... Is> using iseq = std::integer_sequence<int, Is...>;
template <class F, int... Is> DI void sfor_(F&& f, iseq<Is...>) { (f(std::integral_constant<int, Is>{}), ...); }
template <int N, class F> DI void sfor(F&& f) { sfor_(f, std::make_integer_sequence<int, N>{}); }

constexpr int UNIT_B = 9216, REG_B = 4 * UNIT_B, SPARE = 4 * REG_B, LDS_B = 163840;
constexpr int ZERO_O = SPARE + 15360, DUMMY_O = SPARE + 15616;
constexpr size_t WS_CNT = 0, WS_FH = 4096, WS_FKV = 16384, WS_FO = 20480, WS_FB = 28672, WS_RS = 40960, WS_KV = 65536, WS_O = 1u << 20, WS_P = 4u << 20, WS_H = 16u << 20;
constexpr int KV_B = 2816, O_B = 2304;
constexpr u64 FLAGC = 0x4000000000000000ull;
#define PD 5

DI void glds(unsigned lds, unsigned voff, const void* base) {
  asm volatile("s_mov_b32 m0, %0\n\ts_nop 0\n\tglobal_load_lds_dwordx4 %1, %2" ::"s"(lds), "v"(voff), "s"(base));
}
DI void glds1(unsigned lds, unsigned voff, const void* base) {
  asm volatile("s_mov_b32 m0, %0\n\ts_nop 0\n\tglobal_load_lds_dword %1, %2" ::"s"(lds), "v"(voff), "s"(base));
}
DI void ld4(u32x4& d, unsigned voff, const void* base) { asm volatile("global_load_dwordx4 %0, %1, %2 sc1" : "=v"(d) : "v"(voff), "s"(base)); }
DI void ld2(u32x2& d, unsigned voff, const void* base) { asm volatile("global_load_dwordx2 %0, %1, %2 sc1" : "=v"(d) : "v"(voff), "s"(base)); }
DI void ld1(unsigned& d, unsigned voff, const void* base) { asm volatile("global_load_dword %0, %1, %2 sc1" : "=v"(d) : "v"(voff), "s"(base)); }
DI void ldus(unsigned& d, unsigned voff, const void* base) { asm volatile("global_load_ushort %0, %1, %2" : "=v"(d) : "v"(voff), "s"(base)); }
DI void st4(unsigned voff, u32x4 v, void* base) { asm volatile("global_store_dwordx4 %0, %1, %2 sc1\n\ts_nop 1" ::"v"(voff), "v"(v), "s"(base)); }
DI void st2(unsigned voff, u32x2 v, void* base) { asm volatile("global_store_dwordx2 %0, %1, %2 sc1\n\ts_nop 1" ::"v"(voff), "v"(v), "s"(base)); }
DI void st1(unsigned voff, unsigned v, void* base) { asm volatile("global_store_dword %0, %1, %2 sc1\n\ts_nop 1" ::"v"(voff), "v"(v), "s"(base)); }
template <int N> DI void vmw() { asm volatile("s_waitcnt vmcnt(%0)" ::"n"(N < 0 ? 0 : (N > 63 ? 63 : N)) : "memory"); }
template <class T> DI void pin(T& v) { asm volatile("" : "+v"(v)); }
DI void pina(f32x4& v) { asm volatile("" : "+a"(v)); }
DI unsigned pinr(unsigned v) { asm volatile("" : "+v"(v)); return v; }
DI void bar() { asm volatile("" ::: "memory"); __builtin_amdgcn_s_barrier(); asm volatile("" ::: "memory"); }
DI void lbar() { asm volatile("s_waitcnt lgkmcnt(0)" ::: "memory"); __builtin_amdgcn_s_barrier(); asm volatile("" ::: "memory"); }
DI unsigned pk2(float a, float b) { bf16x2 v = {(__bf16)a, (__bf16)b}; return __builtin_bit_cast(unsigned, v); }
DI unsigned relu2(unsigned v) { s16x2 z = {0, 0}; asm("" : "+v"(v)); return __builtin_bit_cast(unsigned, __builtin_elementwise_max(__builtin_bit_cast(s16x2, v), z)); }
DI f32x4 mfma(u32x4 a, u32x4 b, f32x4 c) { return __builtin_amdgcn_mfma_f32_16x16x32_bf16(__builtin_bit_cast(bf16x8, a), __builtin_bit_cast(bf16x8, b), c, 0, 0, 0); }
DI void mfmav(f32x4& d, u32x4 a, u32x4 b, f32x4 c) { asm volatile("v_mfma_f32_16x16x32_bf16 %0, %1, %2, %3" : "=&v"(d) : "v"(a), "v"(b), "v"(c)); }
DI u32x2 trr(unsigned a) { return __builtin_bit_cast(u32x2, __builtin_amdgcn_ds_read_tr16_b64_v4bf16(LDSP(bf16x4, a))); }
DI float max3(float a, float b, float c) { float r; asm("v_max3_f32 %0, %1, %2, %3" : "=v"(r) : "v"(a), "v"(b), "v"(c)); return r; }
DI float min3(float a, float b, float c) { float r; asm("v_min3_f32 %0, %1, %2, %3" : "=v"(r) : "v"(a), "v"(b), "v"(c)); return r; }
DI u32x4 cat(u32x2 a, u32x2 b) { return u32x4{a[0], a[1], b[0], b[1]}; }

constexpr int nsl(int U) { return U < 18 ? 7 : ((U == 42 || U == 43) ? 0 : 9); }
constexpr int SI(int U, int T) { int n = T; for (int u = 0; u < U; ++u) n += nsl(u); return n; }
constexpr int NSLOT = SI(97, 0), TAIL = SI(92, 0);
constexpr int s2u(int I) { int u = 0; while (I >= nsl(u)) { I -= nsl(u); ++u; } return u; }
constexpr int s2t(int I) { return I - SI(s2u(I), 0); }
constexpr int cu(int I) { return I < TAIL ? s2u(I) : 92 + (I - TAIL) % 5; }
constexpr int ct(int I) { return I < TAIL ? s2t(I) : (I - TAIL) / 5; }
constexpr int g1i(int U) { return U < 42 ? U - 18 : U - 20; }
constexpr int g2v(int U) {
  if (U < 65) return U - 56;
  if (U < 77) return (1 + (U - 65) / 4) * 9 + (U - 65) % 4;
  if (U >= 88 && U < 92) return -1;
  int k = U < 88 ? U - 77 : U - 81;
  return (1 + k / 5) * 9 + 4 + k % 5;
}
constexpr int npieces(int ST) { return ST <= 23 ? 9 : (ST == 24 ? 5 : 0); }
constexpr int spc(int ST) { return ST == 10 ? 1 : 3; }
constexpr int BI(int ST) { return SI(4 * ST, 2); }
constexpr int pcs(int I) { int ST = s2u(I) / 4, J = I - BI(ST) - 1; return (J >= 0 && J % spc(ST) == 0 && J / spc(ST) < npieces(ST + 3)) ? J / spc(ST) : -1; }
constexpr int HD = 8;
constexpr int I_Q = SI(18, 0), I_PQ = I_Q + 28, I_SM = I_PQ + 19, SMN = 72, I_VW = SI(25, 0), I_PW0 = BI(9) + 1, I_KV = BI(10), I_KVC = SI(44, 3);
constexpr int I_CA = SI(40, 0), I_CB = SI(56, 0), I_GAT = SI(52, 1), I_SUM = SI(60, 0), I_ND = BI(17), I_OST = I_ND + 30;
constexpr int I_PA = SI(54, 0), I_KA = SI(56, 8), I_PK = SI(50, 1), I_KK = SI(52, 0), I_PB = SI(66, 1), I_KB = SI(68, 8), I_PO = SI(78, 1), I_KO = SI(80, 0);
constexpr int hst(int I) {
  int a = I - I_CA - 9, b = I - I_CB - 5;
  if (a >= 0 && a < 32 && a % 8 < 4 && a % 2 == 0) return (a / 8) * 2 + (a % 8) / 2;
  if (b >= 0 && b < 40 && b % 8 < 4 && b % 2 == 0) return 8 + (b / 8) * 2 + (b % 8) / 2;
  return -1;
}
constexpr int I_HA_LAST = I_CA + 9 + 26, I_HB_LAST = I_CB + 5 + 34;
constexpr int phl(int I) { int U = s2u(I), T = s2t(I); return (U + HD >= 65 && U + HD <= 95 && g2v(U + HD) >= 0 && (T == 1 || T == 5)) ? T / 5 : -1; }
constexpr int pfx(int I) { return (I == SI(8, 0) || I == SI(9, 0) || I == SI(10, 0)) ? s2u(I) - 8 : -1; }
constexpr int pst(int I) { int a = I - TAIL - 9; return (a >= 0 && a % 5 == 0) ? a / 5 : -1; }
constexpr int nv(int I) {
  int n = 0;
  n += pcs(I) >= 0; n += hst(I) >= 0; n += phl(I) >= 0; n += pfx(I) >= 0; n += pst(I) >= 0;
  n += I == I_PA; n += I == I_PK; n += I == I_PB; n += I == I_PO;
  n += (I >= I_GAT && I < I_GAT + 16);
  n += (I >= I_KVC + 3 && I < I_KVC + 6);
  n += (I >= I_OST && I < I_OST + 3);
  n += (I > I_KO && I <= I_KO + 12);
  return n;
}
constexpr int between(int Ia, int Ib) { int n = 0; for (int I = Ia + 1; I < Ib; ++I) n += nv(I); return n; }
constexpr int imin(int a, int b) { return a < b ? a : b; }
constexpr int lastpc(int S) { return BI(S - 3) + 1 + spc(S - 3) * (npieces(S) - 1); }
constexpr int stage_wait(int ST) {
  int here = BI(ST), S1 = ST + 1;
  int n = S1 <= 2 ? between(-1, here) + 9 * (2 - S1) : between(lastpc(S1), here);
  if (ST == 12) n = imin(n, between(I_KVC + 5, here));
  if (ST == 13) n = imin(n, between(I_HA_LAST, here));
  if (ST == 16) n = imin(n, between(I_HB_LAST, here));
  if (ST == 19) n = imin(n, between(I_OST + 2, here));
  return n;
}
static_assert(hst(I_HA_LAST) == 7 && hst(I_HB_LAST) == 17 && I_SM + 2 * SMN + 5 <= BI(10) && I_SUM + 66 <= I_ND && I_KV + 16 <= I_KVC && I_SM + SMN + 49 > BI(9), "sched");
DI void spin(u32x2& f, unsigned voff, const void* base, u64 nb) {
  u32x2 t; unsigned cnt;
  asm volatile("s_mov_b32 %2, 0x40000\n"
               "1%=:\n\tv_lshl_add_u64 %1, %0, 0, %5\n\tv_lshrrev_b64 %1, 7, %1\n\tv_cmp_ne_u64 vcc, 0, %1\n\ts_cbranch_vccz 2%=f\n\t"
               "s_sleep 2\n\tglobal_load_dwordx2 %0, %3, %4 sc1\n\ts_waitcnt vmcnt(0)\n\ts_sub_u32 %2, %2, 1\n\ts_cbranch_scc0 1%=b\n"
               "2%=:"
               : "+v"(f), "=&v"(t), "=&s"(cnt) : "v"(voff), "s"(base), "s"(nb) : "vcc", "scc", "memory");
}
DI void st2m(unsigned voff, u32x2 v, void* base, u64 m) {
  asm volatile("s_mov_b64 exec, %3\n\tglobal_store_dwordx2 %0, %1, %2 sc1\n\ts_mov_b64 exec, -1\n\ts_nop 0" ::"v"(voff), "v"(v), "s"(base), "s"(m));
}
#define STAMPT(i)

__global__ __launch_bounds__(256, 1) void k1(const char* __restrict__ x, const char* __restrict__ wqkv, const char* __restrict__ wphi,
                                             const char* __restrict__ wo, const char* __restrict__ w1, const char* __restrict__ w2, char* ws) {
  extern __shared__ __attribute__((aligned(16))) char smem[];
  const int tid = threadIdx.x, lane = tid & 63, wv = __builtin_amdgcn_readfirstlane(tid >> 6);
  const int bid = blockIdx.x, xc = bid & 7, ix = bid >> 3;
  const int q4 = xc & 3, c = ix & 3, sl = 4 * q4 + c, mt = (xc >> 2) * 8 + (ix >> 2), m8 = mt & 7;
  const int g = lane >> 4, n = lane & 15, q = n >> 2, p4 = lane & 3;
  const unsigned lds0 = (unsigned)(size_t)(__attribute__((address_space(3))) char*)smem;
  const int r0 = (m8 * 2 + c * 5) % 18;
  const unsigned l16 = lane * 16;
  STAMPT(0);
  {
    const char* pc; unsigned len;
    asm volatile("s_getpc_b64 %0\n.Lk1_beg:\n\ts_mov_b32 %1, .Lk1_end-.Lk1_beg" : "=s"(pc), "=s"(len));
#pragma unroll
    for (int i = 0; i < 3; ++i) { unsigned o = (tid + 256 * i) * 128; glds1(lds0 + DUMMY_O, o < len ? o : len, pc); }
  }
  u32x2 tkv = {0, 0};
  unsigned wp[3];
  sfor<3>([&](auto Ic) { constexpr int I = decltype(Ic)::value; int e = tid + 256 * I; ldus(wp[I], (e < 648 ? e : 647) * 2, wphi + sl * 1296); });
  const u64 m0 = __builtin_amdgcn_ballot_w64(tid == 0);
  {
    u32x2 one = {1, 0};
    asm volatile("s_mov_b64 exec, %4\n\tglobal_atomic_add_x2 %0, %1, %2, %3 sc0 sc1\n\ts_mov_b64 exec, -1" : "+v"(tkv) : "v"(0u), "v"(one), "s"(ws + WS_CNT + q4 * 256), "s"(m0));
  }
  {
    const unsigned vx = (lane >> 2) * 1152 + (((lane & 3) ^ g) << 4);
    const char* xb = x + (size_t)(mt * 128 + wv * 32) * 1152;
    sfor<36>([&](auto Ic) {
      constexpr int I = decltype(Ic)::value, U = I / 2, B = I % 2;
      int ks = U + r0; ks -= ks >= 18 ? 18 : 0;
      glds(lds0 + (I / 9) * REG_B + wv * UNIT_B + (I % 9) * 1024, vx, xb + B * 16 * 1152 + ks * 64);
    });
  }
  {
    u32x4 z = {0, 0, 0, 0};
#pragma unroll
    for (int i = 0; i < 4; ++i) *LDSP(u32x4, lds0 + SPARE + (i == 3 && tid >= 208 ? 0 : i * 4096) + tid * 16) = z;
  }
  lbar();
  unsigned rho[9], vq[9], vw[9], pa_[5];
#pragma unroll
  for (int p = 0; p < 9; ++p) {
    unsigned ch = p * 64 + lane, r = ch / 18, cc = ch - r * 18, row = ((r >> 2) & 3) * 8 + (r >> 4) * 4 + (r & 3);
    unsigned c2 = cc > 14 ? 14 : cc, ty = (c2 >= 5) + (c2 >= 10);
    rho[p] = r; vw[p] = row * 9216 + cc * 16; vq[p] = row * 3456 + ty * (1152 - 80) + c2 * 16;
    pin(rho[p]); pin(vw[p]); pin(vq[p]);
  }
#pragma unroll
  for (int k = 0; k < 4; ++k) {
    int e = tid + (k & 1) * 256; e = e > 431 ? 431 : e;
    if (k < 2) { int r = e / 6, j = e - r * 6; pa_[k] = (m8 * 72 + r) * 9216 + (j == 5 ? 572 : j * 128); }
    else { int r = e / 3, j = e - r * 3; pa_[k] = (m8 * 144 + r) * 1152 + j * 140; }
    pin(pa_[k]);
  }
  { int e = tid > 53 ? 53 : tid, r = e / 3, j = e - r * 3; pa_[4] = (m8 * 18 + r) * 1152 + j * 140; pin(pa_[4]); }
  vmw<36>();
  pin(tkv); pin(wp[0]); pin(wp[1]); pin(wp[2]);
  *LDSP(u32x2, lds0 + DUMMY_O + (tid == 0 ? 512 : 520)) = tkv;
#pragma unroll
  for (int k = 0; k < 3; ++k) {
    const int e = tid + 256 * k, cidx = e / 18, f = e - cidx * 18, hl = cidx >= 18;
#pragma unroll
    for (int ty = 0; ty < 2; ++ty) {
      int gi = 9 * ty + (cidx >> 2), tile = gi >> 2;
      unsigned wa = SPARE + (((f >> 2) * 3 + (tile >> 1)) * 64 + 16 * (gi & 3) + 4 * (2 * ty + hl) + (f & 3)) * 16 + (4 * (tile & 1) + (cidx & 3)) * 2;
      *LDSP(unsigned short, lds0 + (e < 648 ? wa : DUMMY_O + 528)) = (unsigned short)wp[k];
    }
  }
  const char* w1b = w1 + sl * 576;
  const char* wqb = wqkv + ((sl * 72) & ~15);
  const char* w2b = w2 + (size_t)q4 * 1152 * 1152 + c * 288;
  const char* wob = wo + q4 * 144 * 1152 + c * 288;
  auto issue = [&](auto STc, auto Pc) {
    constexpr int ST = decltype(STc)::value, P = decltype(Pc)::value;
    const unsigned la = lds0 + (ST % 4) * REG_B + wv * UNIT_B + P * 1024;
    const int u = 4 * ST + wv;
    if constexpr (ST < 4) {
      int ks = u + r0; ks -= ks >= 18 ? 18 : 0;
      glds(la, vq[P], wqb + ks * (32 * 3456));
    } else if constexpr (ST == 4) {
      int ks = (wv < 2 ? u : u - 18) + r0; ks -= ks >= 18 ? 18 : 0;
      glds(la, wv < 2 ? vq[P] : vw[P], wv < 2 ? wqb + ks * (32 * 3456) : w1b + ks * (32 * 9216));
    } else if constexpr (ST < 14) {
      int gi = ST <= 10 ? (u > 41 ? 41 : u) - 18 : u - 20, hf = gi >= 18, ks = gi - 18 * hf + r0; ks -= ks >= 18 ? 18 : 0;
      glds(la, vw[P], w1b + ks * (32 * 9216) + hf * 288);
    } else if constexpr (ST == 22) {
      unsigned r = rho[P];
      glds(la, l16 + r * 864 + ((r >> 2) & 3) * (5 * 1152) - (r >> 4) * (12 * 1152), wob + wv * (36 * 1152) + P * 1024);
    } else if constexpr (ST == 24) {
      unsigned r = rho[P];
      glds(la, l16 + P * 1024 - r * 288 + ((r >> 2) & 3) * (9 * 1152) + (r & 3) * (36 * 1152), wob + 8 * 1152);
    } else {
      if constexpr (ST == 14) { vw[P] = l16 + rho[P] * 864; pin(vw[P]); }
      constexpr unsigned PK = g2v(4 * ST) | g2v(4 * ST + 1) << 8 | g2v(4 * ST + 2) << 16 | g2v(4 * ST + 3) << 24;
      int v = ((PK >> (8 * wv)) & 255) + 9 * c; v -= v >= 36 ? 36 : 0;
      glds(la, vw[P], w2b + v * (32 * 1152) + P * 1024);
    }
  };
  u32x4 xf[18][2];
  float rstd[2];
  {
    f32x4 sm[2] = {f32x4{0, 0, 0, 0}, f32x4{0, 0, 0, 0}};
    const unsigned xr = lds0 + wv * UNIT_B + n * 64 + ((g ^ q) << 4);
    const u32x4 ones = {0x3f803f80u, 0x3f803f80u, 0x3f803f80u, 0x3f803f80u};
    u32x4 id[2];
#pragma unroll
    for (int jj = 0; jj < 2; ++jj)
#pragma unroll
      for (int w = 0; w < 4; ++w) id[jj][w] = (g == q && (w >> 1) == jj && (w & 1) == (p4 >> 1)) ? (0x3f80u << ((p4 & 1) * 16)) : 0u;
    pin(id[0]); pin(id[1]);
    sfor<4>([&](auto Rc) {
      constexpr int R = decltype(Rc)::value;
      vmw<27>();
      sfor<9>([&](auto Pc) { constexpr int I = R * 9 + decltype(Pc)::value; xf[I / 2][I % 2] = *LDSP(u32x4, xr + R * REG_B + (I % 9) * 1024); });
      asm volatile("s_waitcnt lgkmcnt(0)" ::: "memory");
      if constexpr (R < 3) sfor<9>([&](auto Pc) { issue(Rc, Pc); });
      else { glds1(lds0 + DUMMY_O, pa_[0], w1b); glds1(lds0 + DUMMY_O, pa_[1], w1b); }
      sfor<9>([&](auto Pc) { constexpr int I = R * 9 + decltype(Pc)::value; sm[I % 2] = mfma(ones, xf[I / 2][I % 2], sm[I % 2]); });
      SB();
    });
    STAMPT(1);
    f32x4 nm[2], ca[4], cb[4];
    f32x2 ss[2] = {f32x2{0, 0}, f32x2{0, 0}};
#pragma unroll
    for (int b = 0; b < 2; ++b) { float m = sm[b][0] * (-1.f / 576.f); nm[b] = f32x4{m, m, m, m}; }
    asm volatile("s_nop 1" : "+v"(nm[0]), "+v"(nm[1]));
    SB();
    auto fin = [&](const f32x4& v, unsigned& lo, unsigned& hi, f32x2& s) {
      lo = pk2(v[0], v[1]); hi = pk2(v[2], v[3]);
      f32x2 a = {v[0], v[1]}, b = {v[2], v[3]};
      s = __builtin_elementwise_fma(a, a, s); s = __builtin_elementwise_fma(b, b, s);
    };
    sfor<39>([&](auto Ic) {
      constexpr int I = decltype(Ic)::value, K = I - 3;
      unsigned w0, w1_, w2_, w3;
      if constexpr (I < 36) { mfmav(ca[I % 4], id[0], xf[I / 2][I % 2], nm[I % 2]); SB(); } else asm volatile("s_nop 3");
      if constexpr (K >= 0) { fin(ca[K % 4], w0, w1_, ss[K % 2]); SB(); }
      if constexpr (I < 36) { mfmav(cb[I % 4], id[1], xf[I / 2][I % 2], nm[I % 2]); SB(); }
      if constexpr (K >= 0) { fin(cb[K % 4], w2_, w3, ss[K % 2]); xf[K / 2][K % 2] = u32x4{w0, w1_, w2_, w3}; SB(); }
    });
#pragma unroll
    for (int b = 0; b < 2; ++b) rstd[b] = ss[b][0] + ss[b][1];
  }
  vmw<20>();
  lbar();
  STAMPT(2);
  const unsigned rbase = lds0 + (4 * g + q) * 288 + 8 * p4 + (sl & 1) * 8;
  unsigned rb[4], rb2[4], rb4[4];
#pragma unroll
  for (int i = 0; i < 4; ++i) { rb[i] = rbase + i * REG_B; rb2[i] = rb[i] + (p4 >= 1) * 8; rb4[i] = rb[i] + (p4 >= 2) * 8; pin(rb[i]); pin(rb2[i]); pin(rb4[i]); }
  u32x2 alo[PD + 1], ahi[PD + 1];
  auto raddr = [&](auto Ic) -> unsigned {
    constexpr int I = decltype(Ic)::value, U = cu(I), T = ct(I), R = (U / 4) % 4;
    if constexpr (U < 18) return (T == 2 ? rb2[R] : (T == 4 ? rb4[R] : rb[R])) + (U % 4) * UNIT_B + T * 32 + ((4 * T) / 9) * 8;
    else if constexpr (U == 96) return rb2[0] + T * 32;
    else return rb[R] + (U % 4) * UNIT_B + T * 32;
  };
  auto rdlo = [&](auto Ic) { constexpr int I = decltype(Ic)::value; if constexpr (I < NSLOT) alo[I % (PD + 1)] = trr(raddr(Ic)); };
  auto rdhi = [&](auto Ic) {
    constexpr int I = decltype(Ic)::value;
    if constexpr (I < NSLOT) { if constexpr (cu(I) == 96) ahi[I % (PD + 1)] = alo[I % (PD + 1)]; else ahi[I % (PD + 1)] = trr(raddr(Ic) + 16 * 288); }
  };
  const f32x4 z4 = {0, 0, 0, 0};
  f32x4 acc[9][2], oa[9][2], pq[5][2], kv[3], nd[3][2];
  u32x4 hf[9][2], ph[HD + 1][2], phi[5][2], gk[16], of[4][2], qf[4][2], wf[4], bv, lf[2], po;
  u32x2 kw[3], pf;
  unsigned ol[4], nda[3], pwa, kpa, kva, ko;
  float p[18], e[36], mx, mn, sc[2];
  f32x2 cc2, a1, a2, tt, s1, s2, gs[4];
  u64 tk = 0, mk = 0;
  const unsigned pimg = lds0 + 2 * REG_B + 2 * UNIT_B, vimg = lds0 + SPARE, dum = lds0 + DUMMY_O + (lane & 31) * 8;
  char* hw = ws + WS_H + (size_t)((mt * 16 + sl) * 4 + wv) * 18432;
  const char* hr = ws + WS_H + (size_t)((mt * 16 + 4 * q4) * 4 + wv) * 18432;
  char* pw = ws + WS_P + (size_t)(((q4 * 16 + mt) * 4 + c) * 4 + wv) * 9216;
  const int grp = (mt * 4 + q4) * 128;
  auto flag = [&](size_t off) { u64 v = 63 - tk; st2m(0u, u32x2{(unsigned)v, (unsigned)(v >> 32)}, ws + off, m0); };

  sfor<PD>([&](auto I) { rdlo(I); rdhi(I); });
  sfor<NSLOT>([&](auto Ic) {
    constexpr int I = decltype(Ic)::value, U = cu(I), T = ct(I), ST = s2u(I) / 4, HS = hst(I), PF = pfx(I), PH = phl(I), PS = pst(I), GI = g1i(U) % 18;
    if constexpr (I == BI(ST) && ST <= 22) {
      vmw<stage_wait(ST)>();
      if constexpr (ST == 10 || ST == 17) lbar(); else bar();
      if constexpr (ST == 12) flag(WS_FKV + sl * 128 + mt * 8);
      if constexpr (ST == 13) flag(WS_FH + grp + c * 8);
      if constexpr (ST == 16) flag(WS_FB + grp + c * 8);
      if constexpr (ST == 19) flag(WS_FO + grp + c * 8);
    }
    if constexpr (I == I_KK) { vmw<between(I_PK, I)>(); spin(pf, n * 8, ws + WS_FKV + sl * 128, tk); }
    if constexpr (I == I_KA) { vmw<between(I_PA, I)>(); spin(pf, p4 * 8, ws + WS_FH + grp, tk); }
    if constexpr (I == I_KB) { vmw<between(I_PB, I)>(); spin(pf, p4 * 8, ws + WS_FB + grp, tk); }
    if constexpr (I == I_KO) { vmw<between(I_PO, I)>(); spin(pf, p4 * 8, ws + WS_FO + grp, tk); }
    if constexpr (U >= 65 && U < 88 && T == 0) { vmw<between(SI(U - HD, 5), I)>(); pin(ph[U % (HD + 1)][0]); pin(ph[U % (HD + 1)][1]); }
    if constexpr (I == TAIL) { vmw<between(SI(95 - HD, 5), I)>(); sfor<4>([&](auto K) { pin(ph[(92 + K) % (HD + 1)][0]); pin(ph[(92 + K) % (HD + 1)][1]); }); }
    if constexpr (I == SI(91, 0)) vmw<between(lastpc(24), I)>();
    if constexpr (I == I_SUM) { vmw<between(I_GAT + 15, I)>(); sfor<16>([&](auto K) { pin(gk[K]); }); }
    if constexpr (I == SI(88, 0)) { vmw<between(I_KO + 12, I)>(); sfor<4>([&](auto K) { pin(of[K][0]); pin(of[K][1]); pin(ol[K]); }); }
    if constexpr (pcs(I) >= 0) issue(IC(ST + 3), IC(pcs(I)));
    if constexpr (HS >= 0) st4(l16, hf[HS / 2][HS % 2], hw + HS * 1024);
    if constexpr (PF >= 0) glds1(lds0 + DUMMY_O, pa_[2 + PF], PF < 2 ? w2b : wob);
    if constexpr (PH >= 0) {
      constexpr int V = g2v(U + HD);
      ld4(ph[(U + HD) % (HD + 1)][PH], l16, hr + (size_t)((c + V / 9) & 3) * (4 * 18432) + ((V % 9) * 2 + PH) * 1024);
    }
    if constexpr (I == I_PK) ld2(pf, n * 8, ws + WS_FKV + sl * 128);
    if constexpr (I == I_PA) ld2(pf, p4 * 8, ws + WS_FH + grp);
    if constexpr (I == I_PB) ld2(pf, p4 * 8, ws + WS_FB + grp);
    if constexpr (I == I_PO) ld2(pf, p4 * 8, ws + WS_FO + grp);
    if constexpr (I == I_GAT - 1) { int ch = wv * 43 + lane; ko = (lane < 43 && ch < 171 ? ch : 171) * 16; pin(ko); }
    if constexpr (I >= I_GAT && I < I_GAT + 16) ld4(gk[I - I_GAT], ko, ws + WS_KV + (sl * 16 + I - I_GAT) * KV_B);
    if constexpr (I >= I_KVC + 3 && I < I_KVC + 6) { constexpr int K = I - I_KVC - 3; st2m(ko, kw[K], ws + WS_KV + (sl * 16 + mt) * KV_B + K * 32, K == 2 ? mk & 0xffffull : mk); }
    if constexpr (I >= I_OST && I < I_OST + 3) {
      char* ow = ws + WS_O + ((mt * 16 + sl) * 4 + wv) * O_B;
      if constexpr (I < I_OST + 2) st4(l16, of[0][I - I_OST], ow + (I - I_OST) * 1024); else st1(lane * 4, ol[0], ow + 2048);
    }
    if constexpr (I > I_KO && I <= I_KO + 12) {
      constexpr int K = (I - I_KO - 1) / 3, W = (I - I_KO - 1) % 3;
      const char* orr = ws + WS_O + ((mt * 16 + 4 * q4 + K) * 4 + wv) * O_B;
      if constexpr (W < 2) ld4(of[K][W], l16, orr + W * 1024); else ld1(ol[K], lane * 4, orr + 2048);
    }
    if constexpr (I == 3) { u32x2 t = *LDSP(u32x2, lds0 + DUMMY_O + 512); tk = 63 - FLAGC - ((u64)__builtin_amdgcn_readfirstlane(t[1]) << 32 | __builtin_amdgcn_readfirstlane(t[0])); }
    if constexpr (I == 8 || I == 14) { rstd[0] += __shfl_xor(rstd[0], I == 8 ? 16 : 32); rstd[1] += __shfl_xor(rstd[1], I == 8 ? 16 : 32); }
    if constexpr (I == 20) {
#pragma unroll
      for (int b = 0; b < 2; ++b) { rstd[b] = __builtin_amdgcn_rsqf(rstd[b] * (1.f / 576.f) + 1e-5f); sc[b] = g >= 2 ? rstd[b] : 1.f; }
    }
    if constexpr (I == 24) {
      const u64 mr = __builtin_amdgcn_ballot_w64(sl == 0 && lane < 32);
      asm volatile("s_mov_b64 exec, %3\n\tglobal_store_dword %0, %1, %2 sc1\n\ts_mov_b64 exec, -1" ::"v"(lane * 4), "v"(g == 0 ? rstd[0] : rstd[1]), "s"(ws + WS_RS + (mt * 128 + wv * 32) * 4), "s"(mr));
    }
    if constexpr (I == I_Q - PD) {
#pragma unroll
      for (int i = 0; i < 4; ++i) { rb[i] -= (sl & 1) * 8; pin(rb[i]); }
      rb2[0] = rb[0] + wv * UNIT_B; pin(rb2[0]);
    }
    if constexpr (I == I_Q || I == SI(36, 0) || I == I_CB) sfor<9>([&](auto K) { oa[K][0] = acc[K][0]; oa[K][1] = acc[K][1]; });
    if constexpr (I >= I_Q && I < I_Q + 28) {
      constexpr int H = I - I_Q, t = H / 4, b = (H / 2) % 2, hh = H % 2;
      pina(oa[t][b]);
      f32x2 v = {oa[t][b][2 * hh], oa[t][b][2 * hh + 1]};
      if constexpr (t == 4) v *= sc[b];
      if constexpr (t > 4) v *= rstd[b];
      unsigned w = pk2(v[0], v[1]);
      if constexpr (t == 6) w = g == 3 ? (hh ? 0u : 0x3f80u) : w;
      qf[t / 2][b][(t % 2) * 2 + hh] = pinr(w);
    }
    if constexpr (I >= I_PQ + 3 && I < I_PQ + 18) {
      constexpr int K = I - I_PQ - 3, t = K / 3, s = K % 3;
      pq[t][0] = mfma(wf[K % 4], qf[s][0], s == 0 ? z4 : pq[t][0]); pq[t][1] = mfma(wf[K % 4], qf[s][1], s == 0 ? z4 : pq[t][1]);
    }
    if constexpr (I >= I_PQ && I < I_PQ + 15) wf[(I - I_PQ) % 4] = *LDSP(u32x4, lds0 + SPARE + (I - I_PQ) * 1024 + l16);
    if constexpr (I >= I_VW && I < I_VW + 6) {
      constexpr int b = (I - I_VW) / 3, K = (I - I_VW) % 3;
      const unsigned va = vimg + (wv * 32 + b * 16 + n) * 80 + g * 8;
      if constexpr (K == 0) *LDSP(u32x2, g >= 2 ? va - 16 : dum) = u32x2{qf[2][b][0], qf[2][b][1]};
      if constexpr (K == 1) *LDSP(u32x2, va + 16) = u32x2{qf[2][b][2], qf[2][b][3]};
      if constexpr (K == 2) *LDSP(u32x2, va + 48) = u32x2{qf[3][b][0], qf[3][b][1]};
    }
    if constexpr (I >= I_SM && I < I_SM + 2 * SMN) {
      constexpr int b = (I - I_SM) / SMN, k = (I - I_SM) % SMN;
      if constexpr (k < 9) {
        pina(pq[k / 2][b]);
        p[2 * k] = pq[k / 2][b][(k % 2) * 2]; p[2 * k + 1] = pq[k / 2][b][(k % 2) * 2 + 1];
        mx = max3(k ? mx : p[0], p[2 * k], p[2 * k + 1]); mn = min3(k ? mn : p[0], p[2 * k], p[2 * k + 1]); pin(mx); pin(mn);
      } else if constexpr (k == 9) {
        float cs = rstd[b] * 1.44269504f; cc2 = f32x2{cs, cs}; a1 = f32x2{-mx * cs, -mx * cs}; a2 = f32x2{mn * cs, mn * cs}; pin(cc2); pin(a1); pin(a2);
      } else if constexpr (k < 46) {
        constexpr int i = (k - 10) / 4, sub = (k - 10) % 4, o = (sub / 2) * 18 + 2 * i;
        if constexpr (sub == 0) tt = __builtin_elementwise_fma(f32x2{p[2 * i], p[2 * i + 1]}, cc2, a1);
        if constexpr (sub == 2) tt = __builtin_elementwise_fma(f32x2{p[2 * i], p[2 * i + 1]}, -cc2, a2);
        e[o + sub % 2] = __builtin_amdgcn_exp2f(tt[sub % 2]); pin(e[o + sub % 2]);
        if constexpr (sub == 1) { s1 = i ? s1 + f32x2{e[o], e[o + 1]} : f32x2{e[o], e[o + 1]}; pin(s1); }
        if constexpr (sub == 3) { s2 = i ? s2 + f32x2{e[o], e[o + 1]} : f32x2{e[o], e[o + 1]}; pin(s2); }
      } else if constexpr (k == 46) {
        float r1 = __builtin_amdgcn_rcpf(s1[0] + s1[1]), r2 = __builtin_amdgcn_rcpf(s2[0] + s2[1]); s1 = f32x2{r1, r1}; s2 = f32x2{r2, r2}; pin(s1); pin(s2);
        phi[4][b][2] = 0; phi[4][b][3] = 0;
      } else if constexpr (k < 65) {
        constexpr int w = k - 47;
        f32x2 v = f32x2{e[2 * w], e[2 * w + 1]} * (w < 9 ? s1 : s2);
        phi[w / 4][b][w % 4] = pinr(pk2(v[0], v[1]));
      }
      if constexpr (b == 1 && k >= 49 && k <= 65 && (k & 1)) { constexpr int w = (k - 49) / 2; *LDSP(u32x2, pwa + 16 * 144 + w * 8) = u32x2{phi[w / 2][1][(w % 2) * 2], phi[w / 2][1][(w % 2) * 2 + 1]}; }
    }
    if constexpr (I == I_PW0 - 1) { pwa = g >= 2 ? pimg + (wv * 32 + n) * 144 + (g - 2) * 72 : vimg + 10240 + (lane & 31) * 8; pin(pwa); }
    if constexpr (I >= I_PW0 && I < I_PW0 + 9) { constexpr int w = I - I_PW0; *LDSP(u32x2, pwa + w * 8) = u32x2{phi[w / 2][0][(w % 2) * 2], phi[w / 2][0][(w % 2) * 2 + 1]}; }
    if constexpr (I == I_KV - 2) {
      int hl = wv >> 1, dt = wv & 1; pin(hl); pin(dt);
      kpa = pimg + (4 * g + q) * 144 + hl * 72 + p4 * 8; kva = vimg + (4 * g + q) * 80 + (dt ? (p4 == 0 ? 32 : 72) : hl * 40 + p4 * 8);
      int d = dt ? (n == 4 ? 18 : (hl ? n - 2 : n + 16)) : n + 2 * hl;
      mk = __builtin_amdgcn_ballot_w64(dt ? (n == 4 || (n < 4 && (n >> 1) == hl)) : true);
      ko = (hl * 19 + d) * 72 + g * 8; pin(ko); pin(kpa); pin(kva);
    }
    if constexpr (I >= I_KV + 3 && I < I_KV + 15) { constexpr int K = I - I_KV - 3; kv[K % 3] = mfma(wf[K % 4], qf[(K / 3) % 2][0], K < 3 ? z4 : kv[K % 3]); }
    if constexpr (I >= I_KV && I < I_KV + 12) {
      constexpr int K = I - I_KV, ks = K / 3, ft = K % 3;
      if constexpr (ft == 0) qf[ks % 2][0] = cat(trr(kva + ks * 32 * 80), trr(kva + (ks * 32 + 16) * 80));
      wf[K % 4] = cat(trr(kpa + ks * 32 * 144 + ft * 32), trr(kpa + (ks * 32 + 16) * 144 + ft * 32));
    }
    if constexpr (I >= I_KVC && I < I_KVC + 3) { constexpr int K = I - I_KVC; pina(kv[K]); kw[K] = u32x2{pk2(kv[K][0], kv[K][1]), pk2(kv[K][2], kv[K][3])}; pin(kw[K]); }
    if constexpr ((I >= I_CA && I < I_CA + 36) || (I >= I_CB && I < I_CB + 36)) {
      constexpr int H = I - (I >= I_CB ? I_CB : I_CA), t = H / 4, b = (H / 2) % 2, hh = H % 2, G = t + (I >= I_CB ? 9 : 0);
      pina(oa[t][b]);
      hf[G / 2][b][(G % 2) * 2 + hh] = pinr(relu2(pk2(oa[t][b][2 * hh], oa[t][b][2 * hh + 1])));
    }
    if constexpr (I >= I_SUM && I < I_SUM + 64) {
      constexpr int K = (I - I_SUM) / 4, w = (I - I_SUM) % 4;
      f32x2 v = {__uint_as_float(gk[K][w] << 16), __uint_as_float(gk[K][w] & 0xffff0000u)};
      gs[w] = K ? gs[w] + v : v; pin(gs[w]);
    }
    if constexpr (I == I_SUM + 64) *LDSP(u32x4, lds0 + SPARE + ko) = u32x4{pk2(gs[0][0], gs[0][1]), pk2(gs[1][0], gs[1][1]), pk2(gs[2][0], gs[2][1]), pk2(gs[3][0], gs[3][1])};
    if constexpr (I >= I_ND - 3 && I < I_ND) {
      constexpr int t = I - I_ND + 3;
      int dd = t < 2 ? 9 * (q & 1) + 4 * t + p4 : (p4 == 0 ? 9 * (q & 1) + 8 : 18);
      nda[t] = lds0 + ((g == (q >> 1) && (t < 2 || p4 < 2)) ? SPARE + (g * 19 + dd) * 72 : ZERO_O); pin(nda[t]);
    }
    if constexpr (I >= I_ND + 3 && I < I_ND + 18) {
      constexpr int K = I - I_ND - 3, t = (K / 5 + 2) % 3, s = K % 5;
      nd[t][0] = mfma(wf[K % 4], phi[s][0], s == 0 ? z4 : nd[t][0]); nd[t][1] = mfma(wf[K % 4], phi[s][1], s == 0 ? z4 : nd[t][1]);
    }
    if constexpr (I >= I_ND && I < I_ND + 15) {
      constexpr int K = I - I_ND, t = (K / 5 + 2) % 3, s = K % 5;
      u32x2 lo = *LDSP(u32x2, nda[t] + s * 16);
      wf[K % 4] = cat(lo, s == 4 ? lo : *LDSP(u32x2, nda[t] + s * 16 + 8));
    }
    if constexpr (I == I_ND + 19 || I == I_ND + 20) { constexpr int b = I - I_ND - 19; pina(nd[2][b]); sc[b] = __builtin_amdgcn_rcpf((nd[2][b][1] + 1e-6f) * rstd[b]); pin(sc[b]); }
    if constexpr (I >= I_ND + 21 && I < I_ND + 29) {
      constexpr int b = (I - I_ND - 21) / 4, w = (I - I_ND - 21) % 4;
      pina(nd[w / 2][b]);
      f32x2 v = f32x2{nd[w / 2][b][(w % 2) * 2], nd[w / 2][b][(w % 2) * 2 + 1]} * sc[b];
      of[0][b][w] = pinr(pk2(v[0], v[1]));
    }
    if constexpr (I == I_ND + 29) ol[0] = pk2(nd[2][0][0] * sc[0], nd[2][1][0] * sc[1]);
    if constexpr (I == SI(90, 0)) lf[0] = u32x4{__builtin_amdgcn_perm(ol[1], ol[0], 0x05040100), __builtin_amdgcn_perm(ol[3], ol[2], 0x05040100), 0, 0};
    if constexpr (I == SI(90, 1)) lf[1] = u32x4{__builtin_amdgcn_perm(ol[1], ol[0], 0x07060302), __builtin_amdgcn_perm(ol[3], ol[2], 0x07060302), 0, 0};
    if constexpr (I >= TAIL + 5 && U > 92) { constexpr int w = U - 93; pina(acc[T - 1][w / 2]); po[w] = pinr(pk2(acc[T - 1][w / 2][(w % 2) * 2], acc[T - 1][w / 2][(w % 2) * 2 + 1])); }
    if constexpr (PS >= 0) st4(l16, po, pw + PS * 1024);
    SB();
    constexpr bool first = (U == 0 || U == 18 || U == 36 || U == 56);
    auto bop = [&](int b) -> u32x4 {
      if constexpr (U < 18) return xf[U][b];
      else if constexpr (U < 56) return xf[GI][b];
      else if constexpr (U < 65) return hf[U - 56][b];
      else if constexpr (U == 96) return lf[b];
      else if constexpr (g2v(U) < 0) return of[U - 88][b];
      else return ph[U % (HD + 1)][b];
    };
    const u32x4 a = cat(alo[I % (PD + 1)], ahi[I % (PD + 1)]);
    rdlo(IC(I + PD)); SB();
    acc[T][0] = mfma(a, bop(0), first ? z4 : acc[T][0]); SB();
    rdhi(IC(I + PD)); SB();
    acc[T][1] = mfma(a, bop(1), first ? z4 : acc[T][1]); SB();
  });
  STAMPT(6);
  st4(l16, u32x4{pk2(acc[8][0][0], acc[8][0][1]), pk2(acc[8][0][2], acc[8][0][3]), pk2(acc[8][1][0], acc[8][1][1]), pk2(acc[8][1][2], acc[8][1][3])}, pw + 8 * 1024);
  { u32x2 v = {64, 0}; asm volatile("s_mov_b64 exec, %3\n\tglobal_atomic_add_x2 %0, %1, %2 sc1\n\ts_mov_b64 exec, -1" ::"v"(0u), "v"(v), "s"(ws + WS_CNT + q4 * 256), "s"(m0)); }
  asm volatile(".Lk1_end:");
}

__global__ __launch_bounds__(576) void k2(const char* __restrict__ x, const char* __restrict__ ws, char* __restrict__ out) {
  __shared__ __attribute__((aligned(16))) float sm[32 * 148];
  const int tid = threadIdx.x, bid = blockIdx.x, t = tid >> 6, lane = tid & 63, g = lane >> 4, n = lane & 15;
  const int wv = bid & 3, c = (bid >> 2) & 3, mt = bid >> 4;
  const u32x4* p = (const u32x4*)(ws + WS_P + (size_t)bid * 9216) + tid;
  u32x4 v[4];
#pragma unroll
  for (int i = 0; i < 4; ++i) v[i] = p[(size_t)i * (256 * 576)];
  const int row = tid / 18, ch = tid - row * 18;
  const size_t go = (size_t)(mt * 128 + wv * 32 + row) * 1152 + c * 288 + ch * 16;
  const u32x4 xv = *(const u32x4*)(x + go);
  const float* rs = (const float*)(ws + WS_RS) + mt * 128 + wv * 32 + n;
  const float r0 = rs[0], r1 = rs[16];
  float a[8];
#pragma unroll
  for (int j = 0; j < 4; ++j) {
    a[2 * j] = 0.f; a[2 * j + 1] = 0.f;
#pragma unroll
    for (int i = 0; i < 4; ++i) { a[2 * j] += __uint_as_float(v[i][j] << 16); a[2 * j + 1] += __uint_as_float(v[i][j] & 0xffff0000u); }
  }
  *(f32x4*)&sm[n * 148 + t * 16 + g * 4] = f32x4{a[0], a[1], a[2], a[3]} * r0;
  *(f32x4*)&sm[(16 + n) * 148 + t * 16 + g * 4] = f32x4{a[4], a[5], a[6], a[7]} * r1;
  __syncthreads();
  f32x4 s0 = *(const f32x4*)&sm[row * 148 + ch * 8], s1 = *(const f32x4*)&sm[row * 148 + ch * 8 + 4];
  u32x4 o;
  o[0] = pk2(s0[0] + __uint_as_float(xv[0] << 16), s0[1] + __uint_as_float(xv[0] & 0xffff0000u));
  o[1] = pk2(s0[2] + __uint_as_float(xv[1] << 16), s0[3] + __uint_as_float(xv[1] & 0xffff0000u));
  o[2] = pk2(s1[0] + __uint_as_float(xv[2] << 16), s1[1] + __uint_as_float(xv[2] & 0xffff0000u));
  o[3] = pk2(s1[2] + __uint_as_float(xv[3] << 16), s1[3] + __uint_as_float(xv[3] & 0xffff0000u));
  *(u32x4*)(out + go) = o;
}

extern "C" void kernel_launch(void* const* d_in, const int* in_sizes, int n_in, void* d_out, int out_size, void* d_ws, size_t ws_size,
                              hipStream_t stream) {
  (void)hipFuncSetAttribute((const void*)k1, hipFuncAttributeMaxDynamicSharedMemorySize, LDS_B);
  k1<<<256, 256, LDS_B, stream>>>((const char*)d_in[0], (const char*)d_in[1], (const char*)d_in[2], (const char*)d_in[3], (const char*)d_in[5],
                                  (const char*)d_in[6], (char*)d_ws);
  k2<<<256, 576, 0, stream>>>((const char*)d_in[0], (const char*)d_ws, (char*)d_out);
}
```

```cpp
#include <hip/hip_runtime.h>
#include <hip/hip_bf16.h>
#include <utility>
typedef __bf16 bf16x8 __attribute__((ext_vector_type(8)));
typedef __bf16 bf16x4 __attribute__((ext_vector_type(4)));
typedef __bf16 bf16x2 __attribute__((ext_vector_type(2)));
typedef short s16x2 __attribute__((ext_vector_type(2)));
typedef float f32x2 __attribute__((ext_vector_type(2)));
typedef float f32x3 __attribute__((ext_vector_type(3)));
typedef float f32x4 __attribute__((ext_vector_type(4)));
typedef unsigned u32x4 __attribute__((ext_vector_type(4)));
typedef unsigned u32x2 __attribute__((ext_vector_type(2)));
typedef unsigned long long u64;
#define DI __device__ __forceinline__
#define LDSP(T, a) ((__attribute__((address_space(3))) T*)(size_t)(a))
#define IC(v) std::integral_constant<int, (v)>{}
#define SB() __builtin_amdgcn_sched_barrier(0)
template <int... Is> using iseq = std::integer_sequence<int, Is...>;
template <class F, int... Is> DI void sfor_(F&& f, iseq<Is...>) { (f(std::integral_constant<int, Is>{}), ...); }
template <int N, class F> DI void sfor(F&& f) { sfor_(f, std::make_integer_sequence<int, N>{}); }

constexpr int UNIT_B = 9216, REG_B = 4 * UNIT_B, SPARE = 4 * REG_B, LDS_B = 163840;
constexpr int ZERO_O = SPARE + 15360, DUMMY_O = SPARE + 15616;
constexpr size_t WS_CNT = 0, WS_FH = 4096, WS_FKV = 16384, WS_FO = 20480, WS_FB = 28672, WS_RS = 40960, WS_KV = 65536, WS_O = 1u << 20, WS_P = 4u << 20, WS_H = 16u << 20;
constexpr int KV_B = 2816, HW_B = 18432 + 128, O_B = 2304;
constexpr u64 FLAGC = 0x4000000000000000ull;
#define PD 7

DI void glds(unsigned lds, unsigned voff, const void* base) {
  asm volatile("s_mov_b32 m0, %0\n\ts_nop 0\n\tglobal_load_lds_dwordx4 %1, %2" ::"s"(lds), "v"(voff), "s"(base));
}
DI void setm0(unsigned lds) { asm volatile("s_mov_b32 m0, %0" ::"s"(lds)); }
template <int O> DI void gldso(unsigned voff, const void* base) { asm volatile("global_load_lds_dwordx4 %0, %1 offset:%2" ::"v"(voff), "s"(base), "n"(O)); }
DI void pins(const char*& p) { asm volatile("" : "+s"(p)); }
DI void glds1(unsigned lds, unsigned voff, const void* base) {
  asm volatile("s_mov_b32 m0, %0\n\ts_nop 0\n\tglobal_load_lds_dword %1, %2" ::"s"(lds), "v"(voff), "s"(base));
}
template <int O> DI void ld4(u32x4& d, unsigned voff, const void* base) { asm volatile("global_load_dwordx4 %0, %1, %2 offset:%3 sc1" : "=v"(d) : "v"(voff), "s"(base), "n"(O)); }
template <int O> DI void ld3(f32x3& d, unsigned voff, const void* base) { asm volatile("global_load_dwordx3 %0, %1, %2 offset:%3 sc1" : "=v"(d) : "v"(voff), "s"(base), "n"(O)); }
DI void ld2(u32x2& d, unsigned voff, const void* base) { asm volatile("global_load_dwordx2 %0, %1, %2 sc1" : "=v"(d) : "v"(voff), "s"(base)); }
 DI void ld1(unsigned& d, unsigned voff, const void* base) { asm volatile("global_load_dword %0, %1, %2 offset:2048 sc1" : "=v"(d) : "v"(voff), "s"(base)); }
DI void ldus(unsigned& d, unsigned voff, const void* base) { asm volatile("global_load_ushort %0, %1, %2" : "=v"(d) : "v"(voff), "s"(base)); }
template <int O> DI void st4(unsigned voff, u32x4 v, const void* base) { asm volatile("global_store_dwordx4 %0, %1, %2 offset:%3 sc1" ::"v"(voff), "v"(v), "s"(base), "n"(O)); }
DI void st1(unsigned voff, unsigned v, const void* base) { asm volatile("global_store_dword %0, %1, %2 offset:2048 sc1" ::"v"(voff), "v"(v), "s"(base)); }
template <int N> DI void vmw() { asm volatile("s_waitcnt vmcnt(%0)" ::"n"(N < 0 ? 0 : (N > 63 ? 63 : N)) : "memory"); }
template <class T> DI void pin(T& v) { asm volatile("" : "+v"(v)); }
DI void pina(f32x4& v) { asm volatile("" : "+a"(v)); }
DI unsigned pinr(unsigned v) { asm volatile("" : "+v"(v)); return v; }
DI float pinf(float v) { asm volatile("" : "+v"(v)); return v; }
DI float accr(float a) { float r; asm volatile("v_accvgpr_read_b32 %0, %1" : "=v"(r) : "a"(a)); return r; }
DI float accm(float a, float s) { float r; asm volatile("v_accvgpr_read_b32 %0, %1\n\tv_mul_f32 %0, %0, %2" : "=&v"(r) : "a"(a), "v"(s)); return r; }
DI unsigned cvr(float t, float a) { unsigned r; asm volatile("v_accvgpr_read_b32 %0, %2\n\tv_cvt_pk_bf16_f32 %0, %1, %0" : "=&v"(r) : "v"(t), "a"(a)); return r; }
DI unsigned cvrr(float t, float a) { unsigned r; asm volatile("v_accvgpr_read_b32 %0, %2\n\tv_cvt_pk_bf16_f32 %0, %1, %0\n\tv_pk_max_i16 %0, %0, 0" : "=&v"(r) : "v"(t), "a"(a)); return r; }
DI unsigned cvrm(float t, float a, float s) { unsigned r; asm volatile("v_accvgpr_read_b32 %0, %2\n\tv_mul_f32 %0, %0, %3\n\tv_cvt_pk_bf16_f32 %0, %1, %0" : "=&v"(r) : "v"(t), "a"(a), "v"(s)); return r; }
DI unsigned cvm(float t, float a, float s) { unsigned r; asm volatile("v_mul_f32 %0, %2, %3\n\tv_cvt_pk_bf16_f32 %0, %1, %0" : "=&v"(r) : "v"(t), "v"(a), "v"(s)); return r; }
DI void mxn(float& p1, float& mx, float& mn, float p0, float a) {
  asm volatile("v_accvgpr_read_b32 %0, %4\n\tv_max3_f32 %1, %1, %3, %0\n\tv_min3_f32 %2, %2, %3, %0" : "=&v"(p1), "+v"(mx), "+v"(mn) : "v"(p0), "a"(a));
}
DI void bar() { asm volatile("" ::: "memory"); __builtin_amdgcn_s_barrier(); asm volatile("" ::: "memory"); }
DI void lbar() { asm volatile("s_waitcnt lgkmcnt(0)" ::: "memory"); __builtin_amdgcn_s_barrier(); asm volatile("" ::: "memory"); }
DI unsigned pk2(float a, float b) { bf16x2 v = {(__bf16)a, (__bf16)b}; return __builtin_bit_cast(unsigned, v); }
DI unsigned relu2(unsigned v) { s16x2 z = {0, 0}; asm("" : "+v"(v)); return __builtin_bit_cast(unsigned, __builtin_elementwise_max(__builtin_bit_cast(s16x2, v), z)); }
DI f32x4 mfma(u32x4 a, u32x4 b, f32x4 c) { return __builtin_amdgcn_mfma_f32_16x16x32_bf16(__builtin_bit_cast(bf16x8, a), __builtin_bit_cast(bf16x8, b), c, 0, 0, 0); }
DI void mfmav(f32x4& d, u32x4 a, u32x4 b, f32x4 c) { asm volatile("v_mfma_f32_16x16x32_bf16 %0, %1, %2, %3" : "=&v"(d) : "v"(a), "v"(b), "v"(c)); }
DI u32x2 trr(unsigned a) { return __builtin_bit_cast(u32x2, __builtin_amdgcn_ds_read_tr16_b64_v4bf16(LDSP(bf16x4, a))); }
DI float max3(float a, float b, float c) { float r; asm("v_max3_f32 %0, %1, %2, %3" : "=v"(r) : "v"(a), "v"(b), "v"(c)); return r; }
DI float min3(float a, float b, float c) { float r; asm("v_min3_f32 %0, %1, %2, %3" : "=v"(r) : "v"(a), "v"(b), "v"(c)); return r; }
DI u32x4 cat(u32x2 a, u32x2 b) { return u32x4{a[0], a[1], b[0], b[1]}; }

constexpr int nsl(int U) { return U < 18 ? 7 : ((U == 42 || U == 43) ? 0 : 9); }
constexpr int SI(int U, int T) { int n = T; for (int u = 0; u < U; ++u) n += nsl(u); return n; }
constexpr int NSLOT = SI(97, 0), TAIL = SI(92, 0);
constexpr int s2u(int I) { int u = 0; while (u < 99 && I >= nsl(u)) { I -= nsl(u); ++u; } return u; }
constexpr int s2t(int I) { return I - SI(s2u(I), 0); }
constexpr int cu(int I) { return I < TAIL ? s2u(I) : 92 + (I - TAIL) % 5; }
constexpr int ct(int I) { return I < TAIL ? s2t(I) : (I - TAIL) / 5; }
constexpr int g1i(int U) { return U < 42 ? U - 18 : U - 20; }
constexpr int g2v(int U) {
  if (U < 65) return U - 56;
  if (U < 77) return (1 + (U - 65) / 4) * 9 + (U - 65) % 4;
  if (U >= 88 && U < 92) return -1;
  int k = U < 88 ? U - 77 : U - 81;
  return (1 + k / 5) * 9 + 4 + k % 5;
}
constexpr int npieces(int ST) { return ST <= 23 ? 9 : (ST == 24 ? 5 : 0); }
constexpr int spc(int ST) { return ST == 10 ? 1 : 3; }
constexpr int BI(int ST) { return SI(4 * ST, 2); }
constexpr int pcs(int I) { int ST = s2u(I) / 4, J = I - BI(ST) - 1; return (J >= 0 && J % spc(ST) == 0 && J / spc(ST) < npieces(ST + 3)) ? J / spc(ST) : -1; }
constexpr int bst(int I) { int ST = s2u(I) / 4; return (ST > 22) ? 22 : (I >= BI(ST) || ST == 0 ? ST : ST - 1); }
constexpr bool wsl(int I) { return (I - BI(bst(I)) + 3) % 3 == 0; }
constexpr int HD = 8;
constexpr int I_Q = SI(18, 0), I_PQ = I_Q + 28, I_SM = I_PQ + 19, SMN = 72, I_VW = SI(25, 0), I_PW0 = BI(9) + 1, I_KV = BI(10), I_KVC = SI(44, 0);
constexpr int I_CA = SI(36, 0), I_CB = SI(56, 0), I_GAT = SI(59, 2), I_SUM = SI(65, 5), I_ND = BI(17), I_OST = I_ND + 30;
constexpr int I_PA = SI(54, 0), I_KA = SI(56, 8), I_PK = SI(57, 0), I_KK = SI(59, 0), I_PB = SI(70, 1), I_KB = SI(72, 0), I_PO = SI(80, 2), I_KO = SI(82, 0);
constexpr int HSA = 11, HSB = 8;
constexpr int hst(int I) {
  int a = I - I_CA - 17, b = I - I_CB - 5;
  if (a >= 0 && a < 8 * HSA && a % HSA == 0) return a / HSA;
  if (b >= 0 && b < 10 * HSB && b % HSB == 0) return 8 + b / HSB;
  return -1;
}
constexpr int I_HA_LAST = I_CA + 17 + 7 * HSA, I_HB_LAST = I_CB + 5 + 9 * HSB;
constexpr int islot(int U, int b) { return (U >= 77 && U < 81) ? I_KB + 1 + 2 * ((U - 77) * 2 + b) : SI(U - HD, 1 + 4 * b); }
constexpr int phl(int I) { for (int U = 65; U < 96; ++U) for (int b = 0; b < 2; ++b) if (g2v(U) >= 0 && islot(U, b) == I) return 2 * U + b; return -1; }
constexpr int gat(int I) { int a = I - I_GAT; return (a >= 0 && a < 48 && a % 3 == 0) ? a / 3 : -1; }
constexpr int pfx(int I) { int ST = s2u(I) / 4, P = pcs(I + 2); return ST == 2 && (P == 4 || P == 8) ? P / 4 - 1 : (ST == 3 && P == 4 ? 2 : -1); }
constexpr int pst(int I) { int a = I - TAIL - 9; return (a >= 0 && a % 5 == 0) ? a / 5 : -1; }
constexpr int nv(int I) {
  int n = 0;
  n += pcs(I) >= 0; n += hst(I) >= 0; n += phl(I) >= 0; n += pfx(I) >= 0; n += pst(I) >= 0;
  n += I == I_PA; n += I == I_PK; n += I == I_PB; n += I == I_PO;
  n += gat(I) >= 0;
  n += (I >= I_KVC + 3 && I < I_KVC + 6);
  n += (I >= I_OST && I < I_OST + 3);
  n += (I > I_KO && I <= I_KO + 12);
  return n;
}
constexpr int between(int Ia, int Ib) { int n = 0; for (int I = Ia + 1; I < Ib; ++I) n += nv(I); return n; }
constexpr int imin(int a, int b) { return a < b ? a : b; }
constexpr int lastpc(int S) { return BI(S - 3) + 1 + spc(S - 3) * (npieces(S) - 1); }
constexpr int stage_wait(int ST) {
  int here = BI(ST), S1 = ST + 1;
  int n = S1 <= 2 ? between(-1, here) + 9 * (2 - S1) : between(lastpc(S1), here);
  if (ST == 12) n = imin(n, between(I_KVC + 5, here));
  if (ST == 13) n = imin(n, between(I_HA_LAST, here));
  if (ST == 17) n = imin(n, between(I_HB_LAST, here));
  if (ST == 19) n = imin(n, between(I_OST + 2, here));
  return n;
}
static_assert(hst(I_HA_LAST) == 7 && hst(I_HB_LAST) == 17 && I_SM + 2 * SMN + 5 <= BI(10) && I_SUM + 23 <= I_ND && I_HA_LAST < BI(13) && I_HB_LAST < BI(17) && I_KV + 16 <= I_KVC && I_SM + SMN + 49 > BI(9), "sched");
DI void spin(u32x2& f, unsigned voff, const void* base, u64 nb) {
  u32x2 t; unsigned cnt;
  asm volatile("s_mov_b32 %2, 0x40000\n"
               "1%=:\n\tv_lshl_add_u64 %1, %0, 0, %5\n\tv_lshrrev_b64 %1, 7, %1\n\tv_cmp_ne_u64 vcc, 0, %1\n\ts_cbranch_vccz 2%=f\n\t"
               "s_sleep 2\n\tglobal_load_dwordx2 %0, %3, %4 sc1\n\ts_waitcnt vmcnt(0)\n\ts_sub_u32 %2, %2, 1\n\ts_cbranch_scc0 1%=b\n"
               "2%=:"
               : "+v"(f), "=&v"(t), "=&s"(cnt) : "v"(voff), "s"(base), "s"(nb) : "vcc", "scc", "memory");
}
template <int O> DI void st2m(unsigned voff, u32x2 v, const void* base, u64 m) {
  asm volatile("s_mov_b64 exec, %3\n\tglobal_store_dwordx2 %0, %1, %2 offset:%4 sc1\n\ts_mov_b64 exec, -1" ::"v"(voff), "v"(v), "s"(base), "s"(m), "n"(O));
}
#define STAMPT(i)

#define TR(k)
#define PT(k)
__global__ __launch_bounds__(256, 1) void k1(const char* __restrict__ x, const char* __restrict__ wqkv, const char* __restrict__ wphi,
                                             const char* __restrict__ wo, const char* __restrict__ w1, const char* __restrict__ w2, char* ws) {
  extern __shared__ __attribute__((aligned(16))) char smem[];
  const int tid = threadIdx.x, lane = tid & 63, wv = __builtin_amdgcn_readfirstlane(tid >> 6);
  const int bid = blockIdx.x, xc = bid & 7, ix = bid >> 3;
  const int q4 = xc & 3, c = ix & 3, sl = 4 * q4 + c, mt = (xc >> 2) * 8 + (ix >> 2), m8 = mt & 7;
  const int g = lane >> 4, n = lane & 15, q = n >> 2, p4 = lane & 3;
  const unsigned lds0 = (unsigned)(size_t)(__attribute__((address_space(3))) char*)smem;
  const int r0 = (m8 * 2 + c * 5) % 18;
  const unsigned l16 = lane * 16;
  STAMPT(0);
  unsigned trv = 0; TR(62);
  {
    const char* pc; unsigned len;
    asm volatile("s_getpc_b64 %0\n.Lk1_beg:\n\ts_mov_b32 %1, .Lk1_end-.Lk1_beg" : "=s"(pc), "=s"(len));
#pragma unroll
    for (int i = 0; i < 3; ++i) { unsigned o = (tid + 256 * i) * 128; glds1(lds0 + DUMMY_O, o < len ? o : len, pc); }
  }
  const char* wqb = wqkv + ((sl * 72) & ~15);
  PT(2);
  u32x2 tkv = {0, 0};
  unsigned wp[3];
  sfor<3>([&](auto Ic) { constexpr int I = decltype(Ic)::value; int e = tid + 256 * I; ldus(wp[I], (e < 648 ? e : 647) * 2, wphi + sl * 1296); });
  const u64 m0 = __builtin_amdgcn_ballot_w64(tid == 0);
  {
    u32x2 one = {1, 0};
    asm volatile("s_mov_b64 exec, %4\n\tglobal_atomic_add_x2 %0, %1, %2, %3 sc0 sc1\n\ts_mov_b64 exec, -1" : "+v"(tkv) : "v"(0u), "v"(one), "s"(ws + WS_CNT + q4 * 256), "s"(m0));
  }
  {
    const unsigned vx = (lane >> 2) * 1152 + (((lane & 3) ^ g) << 4);
    const char* xb = x + (size_t)(mt * 128 + wv * 32) * 1152;
    sfor<36>([&](auto Ic) {
      constexpr int I = decltype(Ic)::value, U = I / 2, B = I % 2;
      int ks = U + r0; ks -= ks >= 18 ? 18 : 0;
      glds(lds0 + (I / 9) * REG_B + wv * UNIT_B + (I % 9) * 1024, vx, xb + B * 16 * 1152 + ks * 64);
    });
  }
  {
    u32x4 z = {0, 0, 0, 0};
#pragma unroll
    for (int i = 0; i < 4; ++i) *LDSP(u32x4, lds0 + SPARE + (i == 3 && tid >= 208 ? 0 : i * 4096) + tid * 16) = z;
  }
  PT(3);
  lbar();
  PT(4);
  unsigned rho[9], vq[9], vw[9], pa_[5];
#pragma unroll
  for (int p = 0; p < 9; ++p) {
    unsigned ch = p * 64 + lane, r = ch / 18, cc = ch - r * 18, row = ((r >> 2) & 3) * 8 + (r >> 4) * 4 + (r & 3);
    unsigned c2 = cc > 14 ? 14 : cc, ty = (c2 >= 5) + (c2 >= 10);
    rho[p] = r; vw[p] = row * 9216 + cc * 16 - (p % 4) * 1024; vq[p] = row * 3456 + ty * (1152 - 80) + c2 * 16 - (p % 4) * 1024;
    pin(rho[p]); pin(vw[p]); pin(vq[p]);
  }
#pragma unroll
  for (int k = 0; k < 4; ++k) {
    int e = tid + (k & 1) * 256; e = e > 431 ? 431 : e;
    if (k < 2) { int r = e / 6, j = e - r * 6; pa_[k] = (m8 * 72 + r) * 9216 + (j == 5 ? 572 : j * 128); }
    else { int r = e / 3, j = e - r * 3; pa_[k] = (m8 * 144 + r) * 1152 + j * 140; }
    pin(pa_[k]);
  }
  { int e = tid > 53 ? 53 : tid, r = e / 3, j = e - r * 3; pa_[4] = (m8 * 18 + r) * 1152 + j * 140; pin(pa_[4]); }
  PT(5);
  vmw<36>();
  PT(6);
  pin(tkv); pin(wp[0]); pin(wp[1]); pin(wp[2]);
  *LDSP(u32x2, lds0 + DUMMY_O + (tid == 0 ? 512 : 520)) = tkv;
#pragma unroll
  for (int k = 0; k < 3; ++k) {
    const int e = tid + 256 * k, cidx = e / 18, f = e - cidx * 18, hl = cidx >= 18;
#pragma unroll
    for (int ty = 0; ty < 2; ++ty) {
      int gi = 9 * ty + (cidx >> 2), tile = gi >> 2;
      unsigned wa = SPARE + (((f >> 2) * 3 + (tile >> 1)) * 64 + 16 * (gi & 3) + 4 * (2 * ty + hl) + (f & 3)) * 16 + (4 * (tile & 1) + (cidx & 3)) * 2;
      *LDSP(unsigned short, lds0 + (e < 648 ? wa : DUMMY_O + 528)) = (unsigned short)wp[k];
    }
  }
  const char* w1b = w1 + sl * 576;
  const char* w2b = w2 + (size_t)q4 * 1152 * 1152 + c * 288;
  const char* wob = wo + q4 * 144 * 1152 + c * 288;
  const char* cb;
  auto setbase = [&](auto STc) {
    constexpr int ST = decltype(STc)::value;
    const int u = 4 * ST + wv;
    if constexpr (ST < 4) { int ks = u + r0; ks -= ks >= 18 ? 18 : 0; cb = wqb + ks * (32 * 3456); }
    else if constexpr (ST == 4) { int ks = (wv < 2 ? u : u - 18) + r0; ks -= ks >= 18 ? 18 : 0; cb = wv < 2 ? wqb + ks * (32 * 3456) : w1b + ks * (32 * 9216); }
    else if constexpr (ST < 14) { int gi = ST <= 10 ? (u > 41 ? 41 : u) - 18 : u - 20, hf = gi >= 18, ks = gi - 18 * hf + r0; ks -= ks >= 18 ? 18 : 0; cb = w1b + ks * (32 * 9216) + hf * 288; }
    else if constexpr (ST == 22) cb = wob + wv * (36 * 1152);
    else if constexpr (ST == 24) cb = wob + 8 * 1152;
    else {
      constexpr unsigned PK = g2v(4 * ST) | g2v(4 * ST + 1) << 8 | g2v(4 * ST + 2) << 16 | g2v(4 * ST + 3) << 24;
      int v = ((PK >> (8 * wv)) & 255) + 9 * c; v -= v >= 36 ? 36 : 0;
      cb = w2b + v * (32 * 1152);
    }
    pins(cb);
  };
  const unsigned law = lds0 + wv * UNIT_B;
  auto setvo = [&](auto STc, auto Pc) {
    constexpr int ST = decltype(STc)::value, P = decltype(Pc)::value;
    const unsigned r = rho[P];
    if constexpr (ST == 4) vq[P] = pinr(wv < 2 ? vq[P] : vw[P]);
    if constexpr (ST == 14) vw[P] = pinr(l16 + r * 864 + (P / 4) * 4096);
    if constexpr (ST == 22) vq[P] = pinr(vw[P] + ((r >> 2) & 3) * (5 * 1152) - (r >> 4) * (12 * 1152));
    if constexpr (ST == 24) vq[P] = pinr(vw[P] + ((r >> 2) & 3) * (5 * 1152) + (r & 3) * (35 * 1152) - (r >> 4) * (16 * 1152));
  };
  auto issue = [&](auto STc, auto Pc) {
    constexpr int ST = decltype(STc)::value, P = decltype(Pc)::value;
    gldso<(P % 4) * 1024>((ST <= 4 || ST == 22 || ST == 24) ? vq[P] : vw[P], cb);
  };
  u32x4 xf[18][2];
  float rstd[2];
  {
    f32x4 sm[2] = {f32x4{0, 0, 0, 0}, f32x4{0, 0, 0, 0}}, gm[2] = {f32x4{0, 0, 0, 0}, f32x4{0, 0, 0, 0}};
    const unsigned xr = lds0 + wv * UNIT_B + n * 64 + ((g ^ q) << 4);
    const u32x4 ones = {0x3f803f80u, 0x3f803f80u, 0x3f803f80u, 0x3f803f80u};
    u32x4 id[2];
#pragma unroll
    for (int jj = 0; jj < 2; ++jj)
#pragma unroll
      for (int w = 0; w < 4; ++w) id[jj][w] = (g == q && (w >> 1) == jj && (w & 1) == (p4 >> 1)) ? (0x3f80u << ((p4 & 1) * 16)) : 0u;
    pin(id[0]); pin(id[1]);
    sfor<4>([&](auto Rc) {
      constexpr int R = decltype(Rc)::value;
      PT(8 + 2 * R); vmw<27>(); PT(9 + 2 * R);
      sfor<9>([&](auto Pc) { constexpr int I = R * 9 + decltype(Pc)::value; xf[I / 2][I % 2] = *LDSP(u32x4, xr + R * REG_B + (I % 9) * 1024); });
      asm volatile("s_waitcnt lgkmcnt(0)" ::: "memory");
      if constexpr (R < 3) { setbase(Rc); sfor<9>([&](auto Pc) { if constexpr (Pc % 4 == 0) { setm0(law + R * REG_B + Pc * 1024); asm volatile("s_nop 0"); } issue(Rc, Pc); }); }
      else {
        glds1(lds0 + DUMMY_O, pa_[0], w1b); glds1(lds0 + DUMMY_O, pa_[1], w1b);
        glds1(lds0 + DUMMY_O, lane < 4 ? WS_H + ((mt * 16 + 4 * q4 + lane) * 4 + wv) * HW_B + 18432 : (lane == 4 ? WS_O - 128 : WS_H - 128), ws);
      }
      sfor<9>([&](auto Pc) { constexpr int I = R * 9 + decltype(Pc)::value; sm[I % 2] = mfma(ones, xf[I / 2][I % 2], sm[I % 2]); gm[I % 2] = mfma(xf[I / 2][I % 2], xf[I / 2][I % 2], gm[I % 2]); });
      SB();
    });
    STAMPT(1); PT(16);
    f32x4 nm[2], ca[4], cd[4];
#pragma unroll
    for (int b = 0; b < 2; ++b) { float m = sm[b][0] * (-1.f / 576.f); nm[b] = f32x4{m, m, m, m}; }
    asm volatile("s_nop 1" : "+v"(nm[0]), "+v"(nm[1]));
    SB();
    sfor<39>([&](auto Ic) {
      constexpr int I = decltype(Ic)::value, K = I - 3;
      unsigned w0, w1_, w2_, w3;
      if constexpr (I < 36) { mfmav(ca[I % 4], id[0], xf[I / 2][I % 2], nm[I % 2]); SB(); } else asm volatile("s_nop 3");
      if constexpr (K >= 0) { w0 = pk2(ca[K % 4][0], ca[K % 4][1]); w1_ = pk2(ca[K % 4][2], ca[K % 4][3]); SB(); }
      if constexpr (I < 36) { mfmav(cd[I % 4], id[1], xf[I / 2][I % 2], nm[I % 2]); SB(); }
      if constexpr (K >= 0) { w2_ = pk2(cd[K % 4][0], cd[K % 4][1]); w3 = pk2(cd[K % 4][2], cd[K % 4][3]); xf[K / 2][K % 2] = u32x4{w0, w1_, w2_, w3}; SB(); }
    });
#pragma unroll
    for (int b = 0; b < 2; ++b) rstd[b] = (p4 & 2 ? (p4 & 1 ? gm[b][3] : gm[b][2]) : (p4 & 1 ? gm[b][1] : gm[b][0])) * (1.f / 576.f) - nm[b][0] * nm[b][0];
  }
  PT(17);
  PT(18); vmw<18>();
  lbar(); PT(19);
  STAMPT(2);
  const unsigned rbase = lds0 + (4 * g + q) * 288 + 8 * p4 + (sl & 1) * 8;
  unsigned rb[4], rb2[4], rb4[4];
#pragma unroll
  for (int i = 0; i < 4; ++i) { rb[i] = rbase + i * REG_B; rb2[i] = rb[i] + (p4 >= 1) * 8; rb4[i] = rb[i] + (p4 >= 2) * 8; pin(rb[i]); pin(rb2[i]); pin(rb4[i]); }
  u32x2 alo[PD + 1], ahi[PD + 1];
  auto raddr = [&](auto Ic) -> unsigned {
    constexpr int I = decltype(Ic)::value, U = cu(I), T = ct(I), R = (U / 4) % 4;
    if constexpr (U < 18) return (T == 2 ? rb2[R] : (T == 4 ? rb4[R] : rb[R])) + (U % 4) * UNIT_B + T * 32 + ((4 * T) / 9) * 8;
    else if constexpr (U == 96) return rb2[0] + T * 32;
    else return rb[R] + (U % 4) * UNIT_B + T * 32;
  };
  auto rdlo = [&](auto Ic) { constexpr int I = decltype(Ic)::value; if constexpr (I < NSLOT) alo[I % (PD + 1)] = trr(raddr(Ic)); };
  auto rdhi = [&](auto Ic) {
    constexpr int I = decltype(Ic)::value;
    if constexpr (I < NSLOT) { if constexpr (cu(I) == 96) ahi[I % (PD + 1)] = alo[I % (PD + 1)]; else ahi[I % (PD + 1)] = trr(raddr(Ic) + 16 * 288); }
  };
  const f32x4 z4 = {0, 0, 0, 0};
  f32x4 acc[9][2], oa[9][2], pq[5][2], kv[3], nd[3][2];
  u32x4 hf[9][2], ph[HD + 1][2], phi[5][2], of[4][2], qf[4][2], wf[4], lf[2], po;
  u32x4 gk[16], id[2];
  u32x2 pf, kw[3];
  unsigned ol[4], nda[3], pwa, kpa, kva, ko, vwa, vwb, lk[5] = {l16};
  float p[18], e[36], mx, mn, sc[2], cs, a1, a2, tt, t0, t1, s1, s2, gs[6];
  u64 tk = 0, mk = 0;
  const unsigned pimg = lds0 + 2 * REG_B + 2 * UNIT_B, vimg = lds0 + SPARE, dum = lds0 + DUMMY_O + (lane & 31) * 8;
  const char* hp[4];
  const char* pw = ws + WS_P + (size_t)(((q4 * 16 + mt) * 4 + c) * 4 + wv) * 9216;
  const char* kb = ws + WS_KV + sl * 16 * KV_B;
  const char* orr;
  const int grp = (mt * 4 + q4) * 128;
  auto flag = [&](size_t off) { u64 v = 63 - tk; st2m<0>(0u, u32x2{(unsigned)v, (unsigned)(v >> 32)}, ws + off, m0); };

  auto side = [&](auto Ic, auto Hc) {
    constexpr int I = decltype(Ic)::value, U = cu(I), T = ct(I), ST = s2u(I) / 4, HS = hst(I), PF = pfx(I), PH = phl(I), PS = pst(I), PC = pcs(I), PN = pcs(I + 2), SN = s2u(I + 2) / 4 + 3;
    constexpr bool A = decltype(Hc)::value == 0, B = !A;
    if constexpr (A) {
      if constexpr (PF >= 0) glds1(lds0 + DUMMY_O, pa_[2 + PF], PF < 2 ? w2b : wob);
      if constexpr (PN >= 0 && PN % 4 == 0 && (SN != 13 || PN == 0)) setm0(law + (SN % 4) * REG_B + PN * 1024);
      if constexpr (PC >= 0) issue(IC(ST + 3), IC(PC));
      if constexpr (ST == 10 && (PC == 3 || PC == 7)) setm0(law + REG_B + (PC + 1) * 1024);
      if constexpr (HS >= 0) st4<(HS % 4) * 1024>(lk[HS / 4], hf[HS / 2][HS % 2], hp[0]);
      if constexpr (PH >= 0) { constexpr int V = g2v(PH / 2), F = (V % 9) * 2 + PH % 2; ld4<(F % 4) * 1024>(ph[(PH / 2) % (HD + 1)][PH % 2], lk[F / 4], hp[V / 9]); }
      if constexpr (I == I_PK) ld2(pf, n * 8, ws + WS_FKV + sl * 128);
      if constexpr (I == I_PA) ld2(pf, p4 * 8, ws + WS_FH + grp);
      if constexpr (I == I_PB) ld2(pf, p4 * 8, ws + WS_FB + grp);
      if constexpr (I == I_PO) ld2(pf, p4 * 8, ws + WS_FO + grp);
      if constexpr (gat(I) >= 0) { constexpr int K = gat(I); ld4<(K % 2) * KV_B>(gk[K], ko, kb); }
      if constexpr (I >= I_KVC + 3 && I < I_KVC + 6) { constexpr int K = I - I_KVC - 3; st2m<K * 32>(ko, kw[K], kb, K == 2 ? mk & 0xffffull : mk); }
      if constexpr (I >= I_OST && I < I_OST + 3) { if constexpr (I < I_OST + 2) st4<(I - I_OST) * 1024>(l16, of[0][I - I_OST], orr); else st1(lane * 4, ol[0], orr); }
      if constexpr (I > I_KO && I <= I_KO + 12) { constexpr int K = (I - I_KO - 1) / 3, W = (I - I_KO - 1) % 3; if constexpr (W < 2) ld4<W * 1024>(of[K][W], l16, orr); else ld1(ol[K], lane * 4, orr); }
    } else {
      if constexpr (PN == 0) setbase(IC(SN));
      if constexpr (pcs(I + 1) >= 0) setvo(IC(s2u(I + 1) / 4 + 3), IC(pcs(I + 1)));
      if constexpr (gat(I) >= 0 && gat(I) % 2 == 1) ko = pinr(ko + 2 * KV_B);
      if constexpr (I == I_OST - 1) { orr = ws + WS_O + ((mt * 16 + sl) * 4 + wv) * O_B; pins(orr); }
      if constexpr (I >= I_KO && I < I_KO + 12 && (I - I_KO) % 3 == 0) { orr = ws + WS_O + ((mt * 16 + 4 * q4 + (I - I_KO) / 3) * 4 + wv) * O_B; pins(orr); }
      if constexpr (I == I_GAT - 1) { int ch = wv * 43 + lane; ko = pinr(((lane < 43) & (ch < 171) ? ch : 0) * 16); }
      if constexpr (I >= SI(48, 3) && I < SI(48, 6)) { constexpr int K = I - SI(48, 1); lk[K] = pinr(l16 + K * 4096); }
      if constexpr (I == I_CA) lk[1] = pinr(l16 + 4096);
      if constexpr (I >= 30 && I < 34) { hp[I - 30] = ws + WS_H + (size_t)((mt * 16 + 4 * q4 + ((c + I - 30) & 3)) * 4 + wv) * HW_B; pins(hp[I - 30]); }
      if constexpr (I == 3) { u32x2 t = *LDSP(u32x2, lds0 + DUMMY_O + 512); tk = 63 - FLAGC - ((u64)__builtin_amdgcn_readfirstlane(t[1]) << 32 | __builtin_amdgcn_readfirstlane(t[0])); }
      if constexpr (I == 8) { rstd[0] = __shfl(rstd[0], q * 16 + n); rstd[1] = __shfl(rstd[1], q * 16 + n); }
      if constexpr (I == 14 || I == 15) { constexpr int b = I - 14; rstd[b] = pinf(__builtin_amdgcn_rsqf(fmaxf(rstd[b], 0.f) + 1e-5f)); sc[b] = pinf(g >= 2 ? rstd[b] : 1.f); }
      if constexpr (I == 24) {
        const u64 mr = __builtin_amdgcn_ballot_w64(sl == 0 && lane < 32);
        asm volatile("s_mov_b64 exec, %3\n\tglobal_store_dword %0, %1, %2 sc1\n\ts_mov_b64 exec, -1" ::"v"(lane * 4), "v"(g == 0 ? rstd[0] : rstd[1]), "s"(ws + WS_RS + (mt * 128 + wv * 32) * 4), "s"(mr));
      }
      if constexpr (I == I_Q - PD - 1) {
#pragma unroll
        for (int i = 0; i < 4; ++i) { rb[i] -= (sl & 1) * 8; pin(rb[i]); }
        rb2[0] = rb[0] + wv * UNIT_B; pin(rb2[0]);
      }
    }
    if constexpr (I >= I_Q && I < I_Q + 28) {
      constexpr int H = I - I_Q, t = H / 4, b = (H / 2) % 2, hh = H % 2;
      if constexpr (A) t0 = t < 4 ? accr(oa[t][b][2 * hh]) : accm(oa[t][b][2 * hh], t == 4 ? sc[b] : rstd[b]);
      else {
        unsigned w = t < 4 ? cvr(t0, oa[t][b][2 * hh + 1]) : cvrm(t0, oa[t][b][2 * hh + 1], t == 4 ? sc[b] : rstd[b]);
        if constexpr (t == 6) w = g == 3 ? (hh ? 0u : 0x3f80u) : w;
        qf[t / 2][b][(t % 2) * 2 + hh] = w;
      }
    }
    if constexpr (I >= I_PQ + 3 && I < I_PQ + 18) { constexpr int K = I - I_PQ - 3, t = K / 3, s = K % 3; pq[t][B] = mfma(wf[K % 4], qf[s][B], s == 0 ? z4 : pq[t][B]); }
    if constexpr (B && I >= I_PQ && I < I_PQ + 15) wf[(I - I_PQ) % 4] = *LDSP(u32x4, lds0 + SPARE + (I - I_PQ) * 1024 + l16);
    if constexpr (B && I == I_VW - 1) { vwa = pinr(vimg + (wv * 32 + n) * 80 + g * 8); vwb = pinr(g >= 2 ? vwa - 16 : vimg + 10240 + lane * 8); }
    if constexpr (A && I >= I_VW && I < I_VW + 6) {
      constexpr int b = (I - I_VW) / 3, K = (I - I_VW) % 3;
      const unsigned va = vwa + b * 1280;
      if constexpr (K == 0) *LDSP(u32x2, vwb + b * 1280) = u32x2{qf[2][b][0], qf[2][b][1]};
      if constexpr (K == 1) *LDSP(u32x2, va + 16) = u32x2{qf[2][b][2], qf[2][b][3]};
      if constexpr (K == 2) *LDSP(u32x2, va + 48) = u32x2{qf[3][b][0], qf[3][b][1]};
    }
    if constexpr (I >= I_SM && I < I_SM + 2 * SMN) {
      constexpr int b = (I - I_SM) / SMN, k = (I - I_SM) % SMN;
      if constexpr (k < 9) {
        if constexpr (A) { p[2 * k] = accr(pq[k / 2][b][(k % 2) * 2]); if constexpr (k == 0) { mx = -3e38f; mn = 3e38f; } }
        else mxn(p[2 * k + 1], mx, mn, p[2 * k], pq[k / 2][b][(k % 2) * 2 + 1]);
      } else if constexpr (k == 9) {
        if constexpr (A) cs = pinf(rstd[b] * 1.44269504f); else { a1 = pinf(-mx * cs); a2 = pinf(mn * cs); }
      } else if constexpr (k < 47) {
        constexpr int j = k - 10, i = j - 1;
        if constexpr (A && j < 36) tt = pinf(j < 18 ? fmaf(p[j], cs, a1) : fmaf(p[j % 18], -cs, a2));
        if constexpr (B && i >= 1 && i != 18) { if constexpr (i < 18) s1 = pinf((i == 1 ? e[0] : s1) + e[i]); else s2 = pinf((i == 19 ? e[18] : s2) + e[i]); }
        if constexpr (B && j < 36) e[j] = pinf(__builtin_amdgcn_exp2f(tt));
        if constexpr (A && j == 36) s1 = pinf(__builtin_amdgcn_rcpf(s1));
      } else if constexpr (k == 47) {
        if constexpr (A) s2 = pinf(__builtin_amdgcn_rcpf(s2)); else { phi[4][b][2] = 0; phi[4][b][3] = 0; }
      } else if constexpr (k < 66) {
        constexpr int w = k - 48;
        if constexpr (A) t0 = pinf(e[2 * w] * (w < 9 ? s1 : s2)); else phi[w / 4][b][w % 4] = cvm(t0, e[2 * w + 1], w < 9 ? s1 : s2);
      }
      if constexpr (B && b == 1 && k >= 50 && k <= 66 && !(k & 1)) { constexpr int w = (k - 50) / 2; *LDSP(u32x2, pwa + 16 * 144 + w * 8) = u32x2{phi[w / 2][1][(w % 2) * 2], phi[w / 2][1][(w % 2) * 2 + 1]}; }
    }
    if constexpr (B && I == I_PW0 - 2) pwa = pinr(g >= 2 ? pimg + (wv * 32 + n) * 144 + (g - 2) * 72 : vimg + 10240 + (lane & 31) * 8);
    if constexpr (B && I >= I_PW0 && I < I_PW0 + 9) { constexpr int w = I - I_PW0; *LDSP(u32x2, pwa + w * 8) = u32x2{phi[w / 2][0][(w % 2) * 2], phi[w / 2][0][(w % 2) * 2 + 1]}; }
    if constexpr (B && I == 40) {
      int hl = wv >> 1, dt = wv & 1; pin(hl); pin(dt);
      kpa = pimg + (4 * g + q) * 144 + hl * 72 + p4 * 8; kva = vimg + (4 * g + q) * 80 + dt * (p4 == 0 ? 32 : 72) + (1 - dt) * (hl * 40 + p4 * 8);
      int d1 = n == 4 ? 18 : n + 16 - 18 * hl, d = dt ? d1 : n + 2 * hl;
      mk = __builtin_amdgcn_ballot_w64((dt == 0) | (n == 4) | ((n >> 1) == hl));
      ko = (hl * 19 + d) * 72 + g * 8 + mt * KV_B; pin(ko); pin(kpa); pin(kva);
    }
    if constexpr (B && I >= I_KV + 3 && I < I_KV + 15) { constexpr int K = I - I_KV - 3; kv[K % 3] = mfma(wf[K % 4], qf[(K / 3) % 2][0], K < 3 ? z4 : kv[K % 3]); }
    if constexpr (B && I >= I_KV && I < I_KV + 12) {
      constexpr int K = I - I_KV, ks = K / 3, ft = K % 3;
      if constexpr (ft == 0) qf[ks % 2][0] = cat(trr(kva + ks * 32 * 80), trr(kva + (ks * 32 + 16) * 80));
      wf[K % 4] = cat(trr(kpa + ks * 32 * 144 + ft * 32), trr(kpa + (ks * 32 + 16) * 144 + ft * 32));
    }
    if constexpr (I >= I_KVC && I < I_KVC + 3) { constexpr int K = I - I_KVC; kw[K][B] = cvr(accr(kv[K][2 * B]), kv[K][2 * B + 1]); }
    if constexpr ((I >= I_CA && I < I_CA + 72 && (I - I_CA) % 2 == 0) || (I >= I_CB && I < I_CB + 36)) {
      constexpr int H = I >= I_CB ? I - I_CB : (I - I_CA) / 2, t = H / 4, b = (H / 2) % 2, hh = H % 2, G = t + (I >= I_CB ? 9 : 0);
      if constexpr (A) t1 = accr(oa[t][b][2 * hh]); else hf[G / 2][b][(G % 2) * 2 + hh] = cvrr(t1, oa[t][b][2 * hh + 1]);
    }
    if constexpr (B && I >= I_SUM - 8 && I < I_SUM - 6) {
      constexpr int w = I - I_SUM + 8;
      unsigned v = pinr(((g == q) & (w == (p4 >> 1))) ? (0x3f80u << ((p4 & 1) * 16)) : 0u);
      id[0][w] = v; id[0][2 + w] = 0; id[1][w] = 0; id[1][2 + w] = v;
    }
    if constexpr (I >= I_SUM && I < I_SUM + 16) kv[B] = mfma(id[B], gk[I - I_SUM], I == I_SUM ? z4 : kv[B]);
    if constexpr (I >= I_SUM + 18 && I < I_SUM + 20) { constexpr int w = I - I_SUM - 18; po[2 * w + B] = cvr(accr(kv[w][2 * B]), kv[w][2 * B + 1]); }
    if constexpr (B && I == I_SUM + 20) *LDSP(u32x4, lds0 + SPARE - 16 * KV_B + ko) = po;
    if constexpr (B && I >= I_SUM - 5 && I < I_SUM - 2) {
      constexpr int t = I - I_SUM + 5;
      int dd = t < 2 ? 9 * (q & 1) + 4 * t + p4 : (p4 == 0 ? 9 * (q & 1) + 8 : 18);
      nda[t] = pinr(lds0 + ((g == (q >> 1) && (t < 2 || p4 < 2)) ? SPARE + (g * 19 + dd) * 72 : ZERO_O));
    }
    if constexpr (I >= I_ND + 3 && I < I_ND + 18) { constexpr int K = I - I_ND - 3, t = (K / 5 + 2) % 3, s = K % 5; nd[t][B] = mfma(wf[K % 4], phi[s][B], s == 0 ? z4 : nd[t][B]); }
    if constexpr (B && I >= I_ND && I < I_ND + 15) {
      constexpr int K = I - I_ND, t = (K / 5 + 2) % 3, s = K % 5;
      u32x2 lo = *LDSP(u32x2, nda[t] + s * 16);
      wf[K % 4] = cat(lo, s == 4 ? lo : *LDSP(u32x2, nda[t] + s * 16 + 8));
    }
    if constexpr (B && (I == I_ND + 19 || I == I_ND + 20)) { constexpr int b = I - I_ND - 19; sc[b] = pinf(__builtin_amdgcn_rcpf((accr(nd[2][b][1]) + 1e-6f) * rstd[b])); }
    if constexpr (I >= I_ND + 21 && I < I_ND + 29) {
      constexpr int b = (I - I_ND - 21) / 4, w = (I - I_ND - 21) % 4;
      if constexpr (A) t0 = accm(nd[w / 2][b][(w % 2) * 2], sc[b]); else of[0][b][w] = cvrm(t0, nd[w / 2][b][(w % 2) * 2 + 1], sc[b]);
    }
    if constexpr (I == I_ND + 29) { if constexpr (A) t0 = accm(nd[2][0][0], sc[0]); else ol[0] = cvrm(t0, nd[2][1][0], sc[1]); }
    if constexpr (B && I == SI(90, 0)) { lf[0] = u32x4{__builtin_amdgcn_perm(ol[1], ol[0], 0x05040100), __builtin_amdgcn_perm(ol[3], ol[2], 0x05040100), 0, 0}; pin(lf[0]); }
    if constexpr (B && I == SI(90, 1)) { lf[1] = u32x4{__builtin_amdgcn_perm(ol[1], ol[0], 0x07060302), __builtin_amdgcn_perm(ol[3], ol[2], 0x07060302), 0, 0}; pin(lf[1]); }
    if constexpr (I >= TAIL + 5 && U > 92) {
      constexpr int w = U - 93;
      if constexpr (A) t0 = accr(acc[T - 1][w / 2][(w % 2) * 2]); else po[w] = cvr(t0, acc[T - 1][w / 2][(w % 2) * 2 + 1]);
    }
    if constexpr (B && PS >= 0) st4<(PS % 4) * 1024>(lk[PS / 4], po, pw);
  };

  setbase(IC(3)); setm0(law + 3 * REG_B);
  sfor<PD>([&](auto I) { rdlo(I); rdhi(I); });
  sfor<NSLOT>([&](auto Ic) {
    constexpr int I = decltype(Ic)::value, U = cu(I), T = ct(I), ST = s2u(I) / 4, GI = g1i(U) % 18;
    const u32x4 a = cat(alo[I % (PD + 1)], ahi[I % (PD + 1)]);
    rdlo(IC(I + PD)); SB();
    if constexpr (I == BI(ST) && ST <= 22) {
      TR(2 * ST); vmw<stage_wait(ST)>();
      if constexpr (ST == 10 || ST == 17) lbar(); else bar();
      TR(2 * ST + 1);
      if constexpr (ST == 12) flag(WS_FKV + sl * 128 + mt * 8);
      if constexpr (ST == 13) flag(WS_FH + grp + c * 8);
      if constexpr (ST == 17) flag(WS_FB + grp + c * 8);
      if constexpr (ST == 19) flag(WS_FO + grp + c * 8);
    }
    if constexpr (I == I_KK) TR(46);
    if constexpr (I == I_KK) { vmw<between(I_PK, I)>(); spin(pf, n * 8, ws + WS_FKV + sl * 128, tk); }
    if constexpr (I == I_KK) TR(47);
    if constexpr (I == I_KA) TR(48);
    if constexpr (I == I_KA) { vmw<between(I_PA, I)>(); spin(pf, p4 * 8, ws + WS_FH + grp, tk); }
    if constexpr (I == I_KA) TR(49);
    if constexpr (I == I_KB) TR(50);
    if constexpr (I == I_KB) { vmw<between(I_PB, I)>(); spin(pf, p4 * 8, ws + WS_FB + grp, tk); }
    if constexpr (I == I_KB) TR(51);
    if constexpr (I == I_KO) TR(52);
    if constexpr (I == I_KO) { vmw<between(I_PO, I)>(); spin(pf, p4 * 8, ws + WS_FO + grp, tk); }
    if constexpr (I == I_KO) TR(53);
    if constexpr (U >= 65 && U < 88 && T == 0) { vmw<between(islot(U, 1), I)>(); pin(ph[U % (HD + 1)][0]); pin(ph[U % (HD + 1)][1]); }
    if constexpr (I == TAIL) TR(60);
    if constexpr (I == TAIL) { vmw<between(SI(95 - HD, 5), I)>(); sfor<4>([&](auto K) { pin(ph[(92 + K) % (HD + 1)][0]); pin(ph[(92 + K) % (HD + 1)][1]); }); }
    if constexpr (I == TAIL) TR(61);
    if constexpr (I == SI(91, 0)) vmw<between(lastpc(24), I)>();
    if constexpr (I == I_SUM) TR(54);
    if constexpr (I == I_SUM) { vmw<between(I_GAT + 45, I)>(); sfor<16>([&](auto K) { pin(gk[K]); }); }
    if constexpr (I == I_SUM) TR(55);
    if constexpr (I == SI(88, 0)) TR(58);
    if constexpr (I == SI(88, 0)) { vmw<between(I_KO + 12, I)>(); sfor<4>([&](auto K) { pin(of[K][0]); pin(of[K][1]); pin(ol[K]); }); }
    if constexpr (I == SI(88, 0)) TR(59);
    if constexpr (I == I_Q || I == SI(36, 0) || I == I_CB) sfor<9>([&](auto K) { oa[K][0] = acc[K][0]; oa[K][1] = acc[K][1]; });
    if constexpr (wsl(I) && I + PD < NSLOT) __builtin_amdgcn_s_waitcnt(0xC07F | ((I + PD < TAIL ? 2 * PD - 5 : 2 * PD - 7) << 8));
    side(Ic, IC(0)); SB();
    constexpr bool first = (U == 0 || U == 18 || U == 36 || U == 56);
    auto bop = [&](int b) -> u32x4 {
      if constexpr (U < 18) return xf[U][b];
      else if constexpr (U < 56) return xf[GI][b];
      else if constexpr (U < 65) return hf[U - 56][b];
      else if constexpr (U == 96) return lf[b];
      else if constexpr (g2v(U) < 0) return of[U - 88][b];
      else return ph[U % (HD + 1)][b];
    };
    acc[T][0] = mfma(a, bop(0), first ? z4 : acc[T][0]); SB();
    rdhi(IC(I + PD)); SB();
    side(Ic, IC(1)); SB();
    acc[T][1] = mfma(a, bop(1), first ? z4 : acc[T][1]); SB();
  });
  STAMPT(6);
  st4<0>(lk[2], u32x4{pk2(acc[8][0][0], acc[8][0][1]), pk2(acc[8][0][2], acc[8][0][3]), pk2(acc[8][1][0], acc[8][1][1]), pk2(acc[8][1][2], acc[8][1][3])}, pw);
  asm volatile("s_nop 3");
  { u32x2 v = {64, 0}; asm volatile("s_mov_b64 exec, %3\n\tglobal_atomic_add_x2 %0, %1, %2 sc1\n\ts_mov_b64 exec, -1" ::"v"(0u), "v"(v), "s"(ws + WS_CNT + q4 * 256), "s"(m0)); }
  asm volatile(".Lk1_end:");
}

__global__ __launch_bounds__(576) void k2(const char* __restrict__ x, const char* __restrict__ ws, char* __restrict__ out) {
  __shared__ __attribute__((aligned(16))) float sm[32 * 148];
  const int tid = threadIdx.x, bid = blockIdx.x, t = tid >> 6, lane = tid & 63, g = lane >> 4, n = lane & 15;
  const int wv = bid & 3, c = (bid >> 2) & 3, mt = bid >> 4;
  const u32x4* p = (const u32x4*)(ws + WS_P + (size_t)bid * 9216) + tid;
  u32x4 v[4];
#pragma unroll
  for (int i = 0; i < 4; ++i) v[i] = p[(size_t)i * (256 * 576)];
  const int row = tid / 18, ch = tid - row * 18;
  const size_t go = (size_t)(mt * 128 + wv * 32 + row) * 1152 + c * 288 + ch * 16;
  const u32x4 xv = *(const u32x4*)(x + go);
  const float* rs = (const float*)(ws + WS_RS) + mt * 128 + wv * 32 + n;
  const float r0 = rs[0], r1 = rs[16];
  float a[8];
#pragma unroll
  for (int j = 0; j < 4; ++j) {
    a[2 * j] = 0.f; a[2 * j + 1] = 0.f;
#pragma unroll
    for (int i = 0; i < 4; ++i) { a[2 * j] += __uint_as_float(v[i][j] << 16); a[2 * j + 1] += __uint_as_float(v[i][j] & 0xffff0000u); }
  }
  *(f32x4*)&sm[n * 148 + t * 16 + g * 4] = f32x4{a[0], a[1], a[2], a[3]} * r0;
  *(f32x4*)&sm[(16 + n) * 148 + t * 16 + g * 4] = f32x4{a[4], a[5], a[6], a[7]} * r1;
  __syncthreads();
  f32x4 s0 = *(const f32x4*)&sm[row * 148 + ch * 8], s1 = *(const f32x4*)&sm[row * 148 + ch * 8 + 4];
  u32x4 o;
  o[0] = pk2(s0[0] + __uint_as_float(xv[0] << 16), s0[1] + __uint_as_float(xv[0] & 0xffff0000u));
  o[1] = pk2(s0[2] + __uint_as_float(xv[1] << 16), s0[3] + __uint_as_float(xv[1] & 0xffff0000u));
  o[2] = pk2(s1[0] + __uint_as_float(xv[2] << 16), s1[1] + __uint_as_float(xv[2] & 0xffff0000u));
  o[3] = pk2(s1[2] + __uint_as_float(xv[3] << 16), s1[3] + __uint_as_float(xv[3] & 0xffff0000u));
  *(u32x4*)(out + go) = o;
}

extern "C" void kernel_launch(void* const* d_in, const int* in_sizes, int n_in, void* d_out, int out_size, void* d_ws, size_t ws_size,
                              hipStream_t stream) {
  (void)hipFuncSetAttribute((const void*)k1, hipFuncAttributeMaxDynamicSharedMemorySize, LDS_B);
  k1<<<256, 256, LDS_B, stream>>>((const char*)d_in[0], (const char*)d_in[1], (const char*)d_in[2], (const char*)d_in[3], (const char*)d_in[5],
                                  (const char*)d_in[6], (char*)d_ws);
  k2<<<256, 576, 0, stream>>>((const char*)d_in[0], (const char*)d_ws, (char*)d_out);
}
```

```cpp
#include <hip/hip_runtime.h>
#include <hip/hip_bf16.h>
#include <utility>
typedef __bf16 bf16x8 __attribute__((ext_vector_type(8)));
typedef __bf16 bf16x4 __attribute__((ext_vector_type(4)));
typedef __bf16 bf16x2 __attribute__((ext_vector_type(2)));
typedef short s16x2 __attribute__((ext_vector_type(2)));
typedef float f32x2 __attribute__((ext_vector_type(2)));
typedef float f32x3 __attribute__((ext_vector_type(3)));
typedef float f32x4 __attribute__((ext_vector_type(4)));
typedef unsigned u32x4 __attribute__((ext_vector_type(4)));
typedef unsigned u32x2 __attribute__((ext_vector_type(2)));
typedef unsigned long long u64;
#define DI __device__ __forceinline__
#define LDSP(T, a) ((__attribute__((address_space(3))) T*)(size_t)(a))
#define IC(v) std::integral_constant<int, (v)>{}
#define SB() __builtin_amdgcn_sched_barrier(0)
template <int... Is> using iseq = std::integer_sequence<int, Is...>;
template <class F, int... Is> DI void sfor_(F&& f, iseq<Is...>) { (f(std::integral_constant<int, Is>{}), ...); }
template <int N, class F> DI void sfor(F&& f) { sfor_(f, std::make_integer_sequence<int, N>{}); }

constexpr int UNIT_B = 9216, REG_B = 4 * UNIT_B, SPARE = 4 * REG_B, LDS_B = 163840;
constexpr int ZERO_O = SPARE + 15360, DUMMY_O = SPARE + 15616;
constexpr size_t WS_CNT = 0, WS_FH = 4096, WS_FKV = 16384, WS_FO = 20480, WS_FB = 28672, WS_RS = 40960, WS_KV = 65536, WS_O = 1u << 20, WS_P = 4u << 20, WS_H = 16u << 20;
constexpr int KV_B = 2816, HW_B = 18432 + 128, O_B = 2304;
constexpr u64 FLAGC = 0x4000000000000000ull;
#define PD 7

#define XPOL ""
#define WPOL ""
DI void glds(unsigned lds, unsigned voff, const void* base) {
  asm volatile("s_mov_b32 m0, %0\n\ts_nop 0\n\tglobal_load_lds_dwordx4 %1, %2" XPOL ::"s"(lds), "v"(voff), "s"(base));
}
DI void setm0(unsigned lds) { asm volatile("s_mov_b32 m0, %0" ::"s"(lds)); }
template <int O> DI void gldso(unsigned voff, const void* base) { asm volatile("global_load_lds_dwordx4 %0, %1 offset:%2" WPOL ::"v"(voff), "s"(base), "n"(O)); }
DI void pins(const char*& p) { asm volatile("" : "+s"(p)); }
DI void glds1(unsigned lds, unsigned voff, const void* base) {
  asm volatile("s_mov_b32 m0, %0\n\ts_nop 0\n\tglobal_load_lds_dword %1, %2" ::"s"(lds), "v"(voff), "s"(base));
}
template <int O> DI void ld4(u32x4& d, unsigned voff, const void* base) { asm volatile("global_load_dwordx4 %0, %1, %2 offset:%3 sc1" : "=v"(d) : "v"(voff), "s"(base), "n"(O)); }
template <int O> DI void ld3(f32x3& d, unsigned voff, const void* base) { asm volatile("global_load_dwordx3 %0, %1, %2 offset:%3 sc1" : "=v"(d) : "v"(voff), "s"(base), "n"(O)); }
DI void ld2(u32x2& d, unsigned voff, const void* base) { asm volatile("global_load_dwordx2 %0, %1, %2 sc1" : "=v"(d) : "v"(voff), "s"(base)); }
 DI void ld1(unsigned& d, unsigned voff, const void* base) { asm volatile("global_load_dword %0, %1, %2 offset:2048 sc1" : "=v"(d) : "v"(voff), "s"(base)); }
DI void ldus(unsigned& d, unsigned voff, const void* base) { asm volatile("global_load_ushort %0, %1, %2" : "=v"(d) : "v"(voff), "s"(base)); }
template <int O> DI void st4(unsigned voff, u32x4 v, const void* base) { asm volatile("global_store_dwordx4 %0, %1, %2 offset:%3 sc1" ::"v"(voff), "v"(v), "s"(base), "n"(O)); }
#define PPOL " sc1"
template <int O> DI void st4p(unsigned voff, u32x4 v, const void* base) { asm volatile("global_store_dwordx4 %0, %1, %2 offset:%3" PPOL ::"v"(voff), "v"(v), "s"(base), "n"(O)); }
DI void st1(unsigned voff, unsigned v, const void* base) { asm volatile("global_store_dword %0, %1, %2 offset:2048 sc1" ::"v"(voff), "v"(v), "s"(base)); }
template <int N> DI void vmw() { asm volatile("s_waitcnt vmcnt(%0)" ::"n"(N < 0 ? 0 : (N > 63 ? 63 : N)) : "memory"); }
template <class T> DI void pin(T& v) { asm volatile("" : "+v"(v)); }
DI void pina(f32x4& v) { asm volatile("" : "+a"(v)); }
DI unsigned pinr(unsigned v) { asm volatile("" : "+v"(v)); return v; }
DI float pinf(float v) { asm volatile("" : "+v"(v)); return v; }
DI float accr(float a) { float r; asm volatile("v_accvgpr_read_b32 %0, %1" : "=v"(r) : "a"(a)); return r; }
DI float accm(float a, float s) { float r; asm volatile("v_accvgpr_read_b32 %0, %1\n\tv_mul_f32 %0, %0, %2" : "=&v"(r) : "a"(a), "v"(s)); return r; }
DI unsigned cvr(float t, float a) { unsigned r; asm volatile("v_accvgpr_read_b32 %0, %2\n\tv_cvt_pk_bf16_f32 %0, %1, %0" : "=&v"(r) : "v"(t), "a"(a)); return r; }
DI unsigned cvrr(float t, float a) { unsigned r; asm volatile("v_accvgpr_read_b32 %0, %2\n\tv_cvt_pk_bf16_f32 %0, %1, %0\n\tv_pk_max_i16 %0, %0, 0" : "=&v"(r) : "v"(t), "a"(a)); return r; }
DI unsigned cvrm(float t, float a, float s) { unsigned r; asm volatile("v_accvgpr_read_b32 %0, %2\n\tv_mul_f32 %0, %0, %3\n\tv_cvt_pk_bf16_f32 %0, %1, %0" : "=&v"(r) : "v"(t), "a"(a), "v"(s)); return r; }
DI unsigned cvm(float t, float a, float s) { unsigned r; asm volatile("v_mul_f32 %0, %2, %3\n\tv_cvt_pk_bf16_f32 %0, %1, %0" : "=&v"(r) : "v"(t), "v"(a), "v"(s)); return r; }
DI void mxn(float& p1, float& mx, float& mn, float p0, float a) {
  asm volatile("v_accvgpr_read_b32 %0, %4\n\tv_max3_f32 %1, %1, %3, %0\n\tv_min3_f32 %2, %2, %3, %0" : "=&v"(p1), "+v"(mx), "+v"(mn) : "v"(p0), "a"(a));
}
DI void bar() { asm volatile("" ::: "memory"); __builtin_amdgcn_s_barrier(); asm volatile("" ::: "memory"); }
DI void lbar() { asm volatile("s_waitcnt lgkmcnt(0)" ::: "memory"); __builtin_amdgcn_s_barrier(); asm volatile("" ::: "memory"); }
DI unsigned pk2(float a, float b) { bf16x2 v = {(__bf16)a, (__bf16)b}; return __builtin_bit_cast(unsigned, v); }
DI unsigned relu2(unsigned v) { s16x2 z = {0, 0}; asm("" : "+v"(v)); return __builtin_bit_cast(unsigned, __builtin_elementwise_max(__builtin_bit_cast(s16x2, v), z)); }
DI f32x4 mfma(u32x4 a, u32x4 b, f32x4 c) { return __builtin_amdgcn_mfma_f32_16x16x32_bf16(__builtin_bit_cast(bf16x8, a), __builtin_bit_cast(bf16x8, b), c, 0, 0, 0); }
DI void mfmav(f32x4& d, u32x4 a, u32x4 b, f32x4 c) { asm volatile("v_mfma_f32_16x16x32_bf16 %0, %1, %2, %3" : "=&v"(d) : "v"(a), "v"(b), "v"(c)); }
DI u32x2 trr(unsigned a) { return __builtin_bit_cast(u32x2, __builtin_amdgcn_ds_read_tr16_b64_v4bf16(LDSP(bf16x4, a))); }
DI float max3(float a, float b, float c) { float r; asm("v_max3_f32 %0, %1, %2, %3" : "=v"(r) : "v"(a), "v"(b), "v"(c)); return r; }
DI float min3(float a, float b, float c) { float r; asm("v_min3_f32 %0, %1, %2, %3" : "=v"(r) : "v"(a), "v"(b), "v"(c)); return r; }
DI u32x4 cat(u32x2 a, u32x2 b) { return u32x4{a[0], a[1], b[0], b[1]}; }

constexpr int nsl(int U) { return U < 18 ? 7 : ((U == 42 || U == 43) ? 0 : 9); }
constexpr int SI(int U, int T) { return T + (U <= 18 ? 7 * U : 126 + 9 * (U <= 42 ? U - 18 : (U <= 44 ? 24 : U - 20))); }
constexpr int NSLOT = SI(97, 0), NT = 8, TAIL = SI(97 - NT, 0);
constexpr int s2u(int I) { return I < 126 ? (I < 0 ? 0 : I / 7) : (I < 342 ? 18 + (I - 126) / 9 : 44 + (I - 342) / 9); }
constexpr int s2t(int I) { return I - SI(s2u(I), 0); }
constexpr int cu(int I) { return I < TAIL ? s2u(I) : 97 - NT + (I - TAIL) % NT; }
constexpr int ct(int I) { return I < TAIL ? s2t(I) : (I - TAIL) / NT; }
constexpr int g1i(int U) { return U < 42 ? U - 18 : U - 20; }
constexpr int g2v(int U) {
  if (U < 65) return U - 56;
  if (U < 77) return (1 + (U - 65) / 4) * 9 + (U - 65) % 4;
  if (U >= 88 && U < 92) return -1;
  int k = U < 88 ? U - 77 : U - 81;
  return k < 9 ? (1 + k / 3) * 9 + 4 + k % 3 : (1 + (k - 9) / 2) * 9 + 7 + (k - 9) % 2;
}
constexpr int npieces(int ST) { return ST <= 23 ? 9 : (ST == 24 ? 5 : 0); }
constexpr int spc(int ST) { return ST == 10 ? 1 : 3; }
constexpr int BI(int ST) { return SI(4 * ST, 2); }
#define SW 99
constexpr int pslot(int S, int P) { return S < SW ? BI(S - 3) + 1 + spc(S - 3) * P : (S == SW ? BI(S - 3) + 1 + 6 * P : (S < 24 ? BI(S - 2) + 1 + 2 * P : BI(21) + 21 + 2 * P)); }
constexpr int pfind(int I) { int ST = s2u(I) / 4; for (int S = ST + 2; S <= ST + 3; ++S) for (int P = 0; P < npieces(S); ++P) if (S >= 3 && pslot(S, P) == I) return S * 16 + P; return -1; }
constexpr int pcs(int I) { int f = pfind(I); return f < 0 ? -1 : f % 16; }
constexpr int tgt(int I) { return pfind(I) < 0 ? 0 : pfind(I) / 16; }
constexpr int bst(int I) { int ST = s2u(I) / 4; return (ST > 22) ? 22 : (I >= BI(ST) || ST == 0 ? ST : ST - 1); }
constexpr bool wsl(int I) { return (I - BI(bst(I)) + 3) % 3 == 0; }
#define GU 56
#define KAU 59
#define KBD 1
#define PFS 5
constexpr int HD = 8;
constexpr int I_Q = SI(18, 0), I_PQ = I_Q + 28, I_SM = I_PQ + 19, SMN = 72, I_VW = SI(25, 0), I_PW0 = BI(9) + 1, I_KV = BI(10), I_KVC = I_KV + 14, I_KS = BI(11) + 5;
constexpr int I_CA = SI(36, 0), I_CB = SI(56, 0), I_GAT = SI(GU, 6), I_SUM = SI(GU + 7, 2), I_ND = BI(17), I_OST = I_ND + 30;
constexpr int I_PA = SI(KAU - 2, 5), I_KA = SI(KAU, 0), I_PK = SI(GU - 2, 0), I_KK = SI(GU, 4), I_PB = SI(70 + KBD, 3), I_KB = SI(72 + KBD, 0), I_PO = SI(80, 2), I_KO = SI(82, 0);
#define HSA 11
#define HSB 16
constexpr int hst(int I) {
  int a = I - I_CA - 17, b = I - I_CB - 5;
  if (a >= 0 && a < 8 * HSA && a % HSA == 0) return a / HSA;
  if (b >= 0 && b < 10 * HSB && b % HSB == 0) return 8 + b / HSB;
  return -1;
}
constexpr int I_HA_LAST = I_CA + 17 + 7 * HSA, I_HB_LAST = I_CB + 5 + 9 * HSB;
constexpr int islot(int U, int b) { return U < KAU + 8 ? I_KA + 2 + (U - 65) * 2 + b + (U == 66 && b) : (U == 86 || U == 87) ? I_KO + 2 + 2 * ((U - 86) * 2 + b) : (KBD && U >= 77 && U < 82) ? I_KB + (U - 77) * 2 + b : (U >= 77 && U < 81) ? I_KB + 1 + 2 * ((U - 77) * 2 + b) : SI(U - HD, 1 + 4 * b); }
constexpr int phl(int I) { if (I < I_KA || I >= SI(88, 0)) return -1; for (int U = 65; U < 96; ++U) for (int b = 0; b < 2; ++b) if (g2v(U) >= 0 && islot(U, b) == I) return 2 * U + b; return -1; }
constexpr int gat(int I) { int a = I - I_GAT; return (a >= 0 && a < 48 && a % 3 == 0) ? a / 3 : -1; }
constexpr int pfx(int I) { int ST = s2u(I) / 4, P = pcs(I + 2); if (P == 4 && ST >= PFS && ST < PFS + 3) return ST - PFS;
  return -1; }
constexpr int pst(int I) { int a = I - TAIL - NT - 4; return (a >= 0 && a % NT == 0) ? a / NT : -1; }
constexpr int nv(int I) {
  int n = 0;
  n += pcs(I) >= 0; n += hst(I) >= 0; n += phl(I) >= 0; n += pfx(I) >= 0; n += pst(I) >= 0;
  n += I == I_PA; n += I == I_PK; n += I == I_PB; n += I == I_PO;
  n += gat(I) >= 0;
  n += I == I_KS;
  n += (I >= I_OST && I < I_OST + 3);
  n += (I > I_KO && I <= I_KO + 12);
  return n;
}
struct NVT { int s[NSLOT + 2]; constexpr NVT() : s() { for (int I = 0; I <= NSLOT; ++I) s[I + 1] = s[I] + nv(I); } };
constexpr NVT nvt{};
constexpr int between(int Ia, int Ib) { return Ib > Ia + 1 ? nvt.s[Ib] - nvt.s[Ia + 1] : 0; }
constexpr int imin(int a, int b) { return a < b ? a : b; }
constexpr int lastpc(int S) { return pslot(S, npieces(S) - 1); }
constexpr int stage_wait(int ST) {
  int here = BI(ST), S1 = ST + 1;
  int n = S1 <= 2 ? between(-1, here) + 9 * (2 - S1) : between(lastpc(S1), here);
  if (ST == 12) n = imin(n, between(I_KS, here));
  if (ST == 13) n = imin(n, between(I_HA_LAST, here));
  if (ST == 17) n = imin(n, between(I_HB_LAST - 4 * HSB, here));
  if (ST == 19) n = imin(n, imin(between(I_OST + 2, here), between(I_HB_LAST, here)));
  return n;
}
static_assert(hst(I_HA_LAST) == 7 && hst(I_HB_LAST) == 17 && I_SM + 2 * SMN + 5 <= BI(10) && I_SUM + 23 <= I_ND && I_HA_LAST < BI(13) && I_HB_LAST - 4 * HSB < BI(17) && I_HB_LAST < BI(19) && I_KVC + 2 < BI(11) && I_SM + SMN + 49 > BI(9), "sched");
DI void spin(u32x2& f, unsigned voff, const void* base, u64 nb) {
  u32x2 t; unsigned cnt;
  asm volatile("s_mov_b32 %2, 0x40000\n"
               "1%=:\n\tv_lshl_add_u64 %1, %0, 0, %5\n\tv_lshrrev_b64 %1, 7, %1\n\tv_cmp_ne_u64 vcc, 0, %1\n\ts_cbranch_vccz 2%=f\n\t"
               "s_sleep 2\n\tglobal_load_dwordx2 %0, %3, %4 sc1\n\ts_waitcnt vmcnt(0)\n\ts_sub_u32 %2, %2, 1\n\ts_cbranch_scc0 1%=b\n"
               "2%=:"
               : "+v"(f), "=&v"(t), "=&s"(cnt) : "v"(voff), "s"(base), "s"(nb) : "vcc", "scc", "memory");
}
template <int O> DI void st2m(unsigned voff, u32x2 v, const void* base, u64 m) {
  asm volatile("s_mov_b64 exec, %3\n\tglobal_store_dwordx2 %0, %1, %2 offset:%4 sc1\n\ts_mov_b64 exec, -1" ::"v"(voff), "v"(v), "s"(base), "s"(m), "n"(O));
}
template <int O> DI void ld4m(u32x4& d, unsigned voff, const void* base) {
  asm volatile("s_mov_b32 exec_hi, 0x7ff\n\tglobal_load_dwordx4 %0, %1, %2 offset:%3 sc1\n\ts_mov_b32 exec_hi, -1" : "=v"(d) : "v"(voff), "s"(base), "n"(O));
}
template <int O> DI void st2o(unsigned voff, u32x2 v, const void* base, u64 m) {
  asm volatile("s_mov_b64 exec, %3\n\tglobal_store_dwordx2 %0, %1, %2 offset:%4\n\ts_mov_b64 exec, -1" ::"v"(voff), "v"(v), "s"(base), "s"(m), "n"(O));
}
template <int O> DI void ld2x(u32x2& d, unsigned voff, const void* base) { asm volatile("global_load_dwordx2 %0, %1, %2 offset:%3" : "=v"(d) : "v"(voff), "s"(base), "n"(O)); }
#define STAMPT(i)

#define TR(k)
#define PT(k)
#define CPF 4
#define NCP 1
#define VAR 0
__global__ __launch_bounds__(256, 1) void k1(const char* __restrict__ x, const char* __restrict__ wqkv, const char* __restrict__ wphi,
                                             const char* __restrict__ wo, const char* __restrict__ w1, const char* __restrict__ w2, char* ws) {
#ifdef __HIP_DEVICE_COMPILE__
  extern __shared__ __attribute__((aligned(16))) char smem[];
  const int tid = threadIdx.x, lane = tid & 63, wv = __builtin_amdgcn_readfirstlane(tid >> 6);
  const int bid = blockIdx.x, xc = bid & 7, ix = bid >> 3;
  const int q4 = xc & 3, c = ix & 3, sl = 4 * q4 + c, mt = (xc >> 2) * 8 + (ix >> 2), m8 = mt & 7;
  const int g = lane >> 4, n = lane & 15, q = n >> 2, p4 = lane & 3;
  const unsigned lds0 = (unsigned)(size_t)(__attribute__((address_space(3))) char*)smem;
  const int r0 = (m8 * 2 + (c * 5 & 14)) % 18;
  const unsigned l16 = lane * 16;
  STAMPT(0);
  unsigned trv = 0; TR(62);
  const char* wqb = wqkv + ((sl * 72) & ~15);
  PT(2);
  u32x2 tkv = {0, 0};
  unsigned wp[3];
  sfor<3>([&](auto Ic) { constexpr int I = decltype(Ic)::value; int e = tid + 256 * I; ldus(wp[I], (e < 648 ? e : 647) * 2, wphi + sl * 1296); });
  const u64 m0 = __builtin_amdgcn_ballot_w64(tid == 0);
  {
    u32x2 one = {1, 0};
    asm volatile("s_mov_b64 exec, %4\n\tglobal_atomic_add_x2 %0, %1, %2, %3 sc0 sc1\n\ts_mov_b64 exec, -1" : "+v"(tkv) : "v"(0u), "v"(one), "s"(ws + WS_CNT + q4 * 256), "s"(m0));
  }
  const unsigned vx = (lane >> 3) * 1152 + (((lane & 7) ^ (lane >> 3)) << 4);
  const char* xb = x + (size_t)(mt * 128 + wv * 32) * 1152;
  auto xi = [&](auto Ic) {
    constexpr int I = decltype(Ic)::value, O = (I / 18) * 2 + I % 2, U = (I % 18) / 2;
    int ks = 2 * U + r0; ks -= ks >= 18 ? 18 : 0;
    glds(lds0 + O * REG_B + wv * UNIT_B + U * 1024, vx ^ ((O & 1) << 4), xb + O * 8 * 1152 + ks * 64);
  };
  sfor<CPF>(xi);
  {
    const char* pc; unsigned len;
    asm volatile("s_getpc_b64 %0\n.Lk1_beg:\n\ts_mov_b32 %1, .Lk2_end-.Lk1_beg" : "=s"(pc), "=s"(len));
#pragma unroll
    for (int i = 0; i < NCP; ++i) { unsigned o = (tid + 256 * (NCP == 1 ? (c == 3 ? (mt & 1) : c) : i)) * 128; glds1(lds0 + DUMMY_O, o < len ? o : len, pc); }
  }
  sfor<12 - CPF>([&](auto Kc) { xi(IC(CPF + decltype(Kc)::value)); });
  {
    u32x4 z = {0, 0, 0, 0};
#pragma unroll
    for (int i = 0; i < 4; ++i) *LDSP(u32x4, lds0 + SPARE + (i == 3 && tid >= 208 ? 0 : i * 4096) + tid * 16) = z;
  }
  lbar();
  unsigned rho[9], vq[9], vw[9], pa_[5];
  sfor<9>([&](auto Pc) {
    constexpr int p = decltype(Pc)::value;
    unsigned ch = p * 64 + lane, r = ch / 18, cc = ch - r * 18, row = ((r >> 2) & 3) * 8 + (r >> 4) * 4 + (r & 3);
    unsigned c2 = cc > 14 ? 14 : cc, ty = (c2 >= 5) + (c2 >= 10);
    rho[p] = r; vw[p] = row * 9216 + cc * 16 - (p % 4) * 1024; vq[p] = row * 3456 + ty * (1152 - 80) + c2 * 16 - (p % 4) * 1024;
    pin(rho[p]); pin(vw[p]); pin(vq[p]);
    xi(IC(12 + p));
  });
  sfor<4>([&](auto Kc) {
    constexpr int k = decltype(Kc)::value;
    int e = tid + (k & 1) * 256; e = e > 431 ? 431 : e;
    if (k < 2) { int r = e / 6, j = e - r * 6; pa_[k] = (m8 * 72 + r) * 9216 + (j == 5 ? 572 : j * 128); }
    else { int r = e / 3, j = e - r * 3; pa_[k] = (m8 * 144 + r) * 1152 + j * 140; }
    pin(pa_[k]);
    xi(IC(21 + k));
  });
  { int e = tid > 53 ? 53 : tid, r = e / 3, j = e - r * 3; pa_[4] = (m8 * 18 + r) * 1152 + j * 140; pin(pa_[4]); }
  vmw<25>();
  pin(tkv); pin(wp[0]); pin(wp[1]); pin(wp[2]);
  *LDSP(u32x2, lds0 + DUMMY_O + (tid == 0 ? 512 : 520)) = tkv;
  sfor<3>([&](auto Kc) {
    constexpr int k = decltype(Kc)::value;
    const int e = tid + 256 * k, cidx = e / 18, f = e - cidx * 18, hl = cidx >= 18;
#pragma unroll
    for (int ty = 0; ty < 2; ++ty) {
      int gi = 9 * ty + (cidx >> 2), tile = gi >> 2;
      unsigned wa = SPARE + (((f >> 2) * 3 + (tile >> 1)) * 64 + 16 * (gi & 3) + 4 * (2 * ty + hl) + (f & 3)) * 16 + (4 * (tile & 1) + (cidx & 3)) * 2;
      *LDSP(unsigned short, lds0 + (e < 648 ? wa : DUMMY_O + 528)) = (unsigned short)wp[k];
    }
    SB(); xi(IC(25 + k));
  });
  sfor<8>([&](auto Kc) { xi(IC(28 + decltype(Kc)::value)); });
  PT(3);
  const char* w1b = w1 + sl * 576;
  const char* w2b = w2 + (size_t)q4 * 1152 * 1152 + c * 288;
  const char* wob = wo + q4 * 144 * 1152 + c * 288;
  const char* cb;
  auto setbase = [&](auto STc) {
    constexpr int ST = decltype(STc)::value;
    const int u = 4 * ST + wv;
    if constexpr (ST < 4) { int ks = u + r0; ks -= ks >= 18 ? 18 : 0; cb = wqb + ks * (32 * 3456); }
    else if constexpr (ST == 4) { int ks = (wv < 2 ? u : u - 18) + r0; ks -= ks >= 18 ? 18 : 0; cb = wv < 2 ? wqb + ks * (32 * 3456) : w1b + ks * (32 * 9216); }
    else if constexpr (ST < 14) { int gi = ST <= 10 ? (u > 41 ? 41 : u) - 18 : u - 20, hf = gi >= 18, ks = gi - 18 * hf + r0; ks -= ks >= 18 ? 18 : 0; cb = w1b + ks * (32 * 9216) + hf * 288; }
    else if constexpr (ST == 22) cb = wob + wv * (36 * 1152);
    else if constexpr (ST == 24) cb = wob + 8 * 1152;
    else {
      constexpr unsigned PK = g2v(4 * ST) | g2v(4 * ST + 1) << 8 | g2v(4 * ST + 2) << 16 | g2v(4 * ST + 3) << 24;
      int v = ((PK >> (8 * wv)) & 255) + 9 * c; v -= v >= 36 ? 36 : 0;
      cb = w2b + v * (32 * 1152);
    }
    pins(cb);
  };
  const unsigned law = lds0 + wv * UNIT_B;
  auto setvo = [&](auto STc, auto Pc) {
    constexpr int ST = decltype(STc)::value, P = decltype(Pc)::value;
    const unsigned r = rho[P];
    if constexpr (ST == 4) vq[P] = pinr(wv < 2 ? vq[P] : vw[P]);
    if constexpr (ST == 14) vw[P] = pinr(l16 + r * 864 + (P / 4) * 4096);
    if constexpr (ST == 22) vq[P] = pinr(vw[P] + ((r >> 2) & 3) * (5 * 1152) - (r >> 4) * (12 * 1152));
    if constexpr (ST == 24) vq[P] = pinr(vw[P] + ((r >> 2) & 3) * (5 * 1152) + (r & 3) * (35 * 1152) - (r >> 4) * (16 * 1152));
  };
  auto issue = [&](auto STc, auto Pc) {
    constexpr int ST = decltype(STc)::value, P = decltype(Pc)::value;
    gldso<(P % 4) * 1024>((ST <= 4 || ST == 22 || ST == 24) ? vq[P] : vw[P], cb);
  };
  u32x4 xf[18][2];
  float rstd[2];
  {
    f32x4 sm[2] = {f32x4{0, 0, 0, 0}, f32x4{0, 0, 0, 0}}, gm[2] = {f32x4{0, 0, 0, 0}, f32x4{0, 0, 0, 0}};
    const unsigned xr0 = lds0 + wv * UNIT_B + (n >> 3) * REG_B + (n & 7) * 128 + ((g ^ (n & 7) ^ (n >> 3)) << 4), xr1 = xr0 ^ 64;
    const u32x4 ones = {0x3f803f80u, 0x3f803f80u, 0x3f803f80u, 0x3f803f80u};
    u32x4 id[2];
#pragma unroll
    for (int jj = 0; jj < 2; ++jj)
#pragma unroll
      for (int w = 0; w < 4; ++w) id[jj][w] = (g == q && (w >> 1) == jj && (w & 1) == (p4 >> 1)) ? (0x3f80u << ((p4 & 1) * 16)) : 0u;
    pin(id[0]); pin(id[1]);
    f32x4 nm[2], ca[4], cd[4];
    auto piece = [&](auto Sc, auto Pc) { if constexpr (Pc == 0) setbase(Sc); if constexpr (Pc % 4 == 0) { setm0(law + Sc * REG_B + Pc * 1024); asm volatile("s_nop 0"); } issue(Sc, Pc); SB(); };
    sfor<2>([&](auto Rc) {
      constexpr int R = decltype(Rc)::value;
      PT(8 + 4 * R); vmw<18>(); PT(9 + 4 * R);
      sfor<18>([&](auto Pc) { constexpr int U = decltype(Pc)::value; xf[U][R] = *LDSP(u32x4, (U % 2 ? xr1 : xr0) + 2 * R * REG_B + (U / 2) * 1024); });
      asm volatile("s_waitcnt lgkmcnt(0)" ::: "memory");
      sfor<9>([&](auto Pc) {
        piece(IC(2 * R), Pc);
        sfor<2>([&](auto Kc) { constexpr int V = Pc * 2 + decltype(Kc)::value; sm[R] = mfma(ones, xf[V][R], sm[R]); gm[R] = mfma(xf[V][R], xf[V][R], gm[R]); });
        SB();
      });
      PT(10 + 4 * R);
      { float m = sm[R][0] * (-1.f / 576.f); nm[R] = f32x4{m, m, m, m}; }
      asm volatile("s_nop 1" : "+v"(nm[R]));
      SB();
      sfor<21>([&](auto Ic) {
        constexpr int I = decltype(Ic)::value, K = I - 3;
        unsigned w0, w1_, w2_, w3;
        if constexpr (R == 0 && I % 2 == 0 && I < 18) piece(IC(1), IC(I / 2));
        if constexpr (VAR != 1 && R == 1 && (I == 2 || I == 8)) glds1(lds0 + DUMMY_O, pa_[I / 8], w1b);
        if constexpr (R == 1 && I == 14) { unsigned ta = pinr(WS_H + ((mt * 16 + 4 * q4 + lane) * 4 + wv) * HW_B + 18432); unsigned tb = pinr(WS_P + (((q4 * 16 + mt) * 4 + c) * 4 + wv) * 9216); glds1(lds0 + DUMMY_O, lane < 4 ? ta : (lane == 4 ? WS_O - 128 : tb), ws); }
        if constexpr (I < 18) { mfmav(ca[I % 4], id[0], xf[I][R], nm[R]); SB(); } else asm volatile("s_nop 3");
        if constexpr (K >= 0) { w0 = pk2(ca[K % 4][0], ca[K % 4][1]); w1_ = pk2(ca[K % 4][2], ca[K % 4][3]); SB(); }
        if constexpr (I < 18) { mfmav(cd[I % 4], id[1], xf[I][R], nm[R]); SB(); }
        if constexpr (K >= 0) { w2_ = pk2(cd[K % 4][0], cd[K % 4][1]); w3 = pk2(cd[K % 4][2], cd[K % 4][3]); xf[K][R] = u32x4{w0, w1_, w2_, w3}; SB(); }
      });
      PT(11 + 4 * R);
    });
    STAMPT(1);
#pragma unroll
    for (int b = 0; b < 2; ++b) rstd[b] = (p4 & 2 ? (p4 & 1 ? gm[b][3] : gm[b][2]) : (p4 & 1 ? gm[b][1] : gm[b][0])) * (1.f / 576.f) - nm[b][0] * nm[b][0];
  }
  PT(17);
  PT(18); vmw<18>();
  lbar(); PT(19);
  STAMPT(2);
  const unsigned rbase = lds0 + (4 * g + q) * 288 + 8 * p4 + (sl & 1) * 8;
  unsigned rb[4], rb2[4], rb4[4];
#pragma unroll
  for (int i = 0; i < 4; ++i) { rb[i] = rbase + i * REG_B; rb2[i] = rb[i] + (p4 >= 1) * 8; rb4[i] = rb[i] + (p4 >= 2) * 8; pin(rb[i]); pin(rb2[i]); pin(rb4[i]); }
  u32x2 alo[PD + 1], ahi[PD + 1];
  auto raddr = [&](auto Ic) -> unsigned {
    constexpr int I = decltype(Ic)::value, U = cu(I), T = ct(I), R = (U / 4) % 4;
    if constexpr (U < 18) return (T == 2 ? rb2[R] : (T == 4 ? rb4[R] : rb[R])) + (U % 4) * UNIT_B + T * 32 + ((4 * T) / 9) * 8;
    else if constexpr (U == 96) return rb2[0] + T * 32;
    else return rb[R] + (U % 4) * UNIT_B + T * 32;
  };
  auto rdlo = [&](auto Ic) { constexpr int I = decltype(Ic)::value; if constexpr (I < NSLOT) alo[I % (PD + 1)] = trr(raddr(Ic)); };
  auto rdhi = [&](auto Ic) {
    constexpr int I = decltype(Ic)::value;
    if constexpr (I < NSLOT) { if constexpr (cu(I) == 96) ahi[I % (PD + 1)] = alo[I % (PD + 1)]; else ahi[I % (PD + 1)] = trr(raddr(Ic) + 16 * 288); }
  };
  const f32x4 z4 = {0, 0, 0, 0};
  f32x4 acc[9][2], oa[9][2], pq[5][2], kv[3], nd[3][2];
  u32x4 hf[9][2], ph[HD + 1][2], phi[5][2], of[4][2], qf[4][2], wf[4], lf[2], po;
  u32x4 gk[16], id[2];
  u32x2 pf, kw[3];
  unsigned tv, tu, ol[4], nda[3], pwa, kpa, kva, ko, vwa, vwb, lk[5] = {l16};
  float p[18], e[36], mx, mn, sc[2], cs, a1, a2, tt, t0, t1, t2, s1, s2, gs[6];
  u64 tk = 0, mk = 0;
  const unsigned pimg = lds0 + 2 * REG_B + 2 * UNIT_B, vimg = lds0 + SPARE, dum = lds0 + DUMMY_O + (lane & 31) * 8;
  const char* hp[4];
  const char* pw = ws + WS_P + (size_t)(((q4 * 16 + mt) * 4 + c) * 4 + wv) * 9216;
  const char* kb = ws + WS_KV + sl * 16 * KV_B;
  const char* orr;
  const int grp = (mt * 4 + q4) * 128;
  auto flag = [&](size_t off) { u64 v = 63 - tk; st2m<0>(0u, u32x2{(unsigned)v, (unsigned)(v >> 32)}, ws + off, m0); };

  auto side = [&](auto Ic, auto Hc) {
    constexpr int I = decltype(Ic)::value, U = cu(I), T = ct(I), ST = s2u(I) / 4, HS = hst(I), PF = pfx(I), PH = phl(I), PS = pst(I), PC = pcs(I), PN = pcs(I + 2), SN = tgt(I + 2), P1 = pcs(I + 1), S1 = tgt(I + 1);
    constexpr bool A = decltype(Hc)::value == 0, B = !A;
    if constexpr (A) {
      if constexpr (PF >= 0 && PF < 3) glds1(lds0 + DUMMY_O, pa_[2 + PF], PF < 2 ? w2b : wob);
      if constexpr (PC >= 0) issue(IC(tgt(I)), IC(PC));
      if constexpr (P1 >= 0 && P1 % 4 == 0) setm0(law + (S1 % 4) * REG_B + P1 * 1024);
      if constexpr (HS >= 0) st4<(HS % 4) * 1024>(lk[HS / 4], hf[HS / 2][HS % 2], hp[0]);
      if constexpr (PH >= 0) { constexpr int V = g2v(PH / 2), F = (V % 9) * 2 + PH % 2; ld4<(F % 4) * 1024>(ph[(PH / 2) % (HD + 1)][PH % 2], lk[F / 4], hp[V / 9]); }
      if constexpr (I == I_PK) ld2(pf, n * 8, ws + WS_FKV + sl * 128);
      if constexpr (I == I_PA) ld2(pf, p4 * 8, ws + WS_FH + grp);
      if constexpr (I == I_PB) ld2(pf, p4 * 8, ws + WS_FB + grp);
      if constexpr (I == I_PO) ld2(pf, p4 * 8, ws + WS_FO + grp);
      if constexpr (gat(I) >= 0) { constexpr int K = gat(I); ld4<(K % 2) * KV_B>(gk[K], ko, kb); }
      if constexpr (I == I_KS) st4<0>(ko, po, kb);
      if constexpr (I >= I_OST && I < I_OST + 3) { if constexpr (I < I_OST + 2) st4<(I - I_OST) * 1024>(l16, of[0][I - I_OST], orr); else st1(lane * 4, ol[0], orr); }
      if constexpr (I > I_KO && I <= I_KO + 12) { constexpr int K = (I - I_KO - 1) / 3, W = (I - I_KO - 1) % 3; if constexpr (W < 2) ld4<W * 1024>(of[K][W], l16, orr); else ld1(ol[K], lane * 4, orr); }
    } else {
      if constexpr (PN == 0) setbase(IC(SN));
      if constexpr (P1 >= 0) setvo(IC(S1), IC(P1));
      if constexpr (gat(I) >= 0 && gat(I) % 2 == 1) ko = pinr(min(ko + 2 * KV_B, ko - 14 * KV_B));
      if constexpr (I == I_OST - 1) { orr = ws + WS_O + ((mt * 16 + sl) * 4 + wv) * O_B; pins(orr); }
      if constexpr (I >= I_KO && I < I_KO + 12 && (I - I_KO) % 3 == 0) { orr = ws + WS_O + ((mt * 16 + 4 * q4 + (I - I_KO) / 3) * 4 + wv) * O_B; pins(orr); }
      if constexpr (I == I_GAT - 1) { int ch = wv * 43 + lane; ko = pinr(((lane < 43) & (ch < 171) ? ch : 0) * 16 + m8 * 2 * KV_B); }
      if constexpr (I >= SI(48, 3) && I < SI(48, 6)) { constexpr int K = I - SI(48, 1); lk[K] = pinr(l16 + K * 4096); }
      if constexpr (I == I_CA) lk[1] = pinr(l16 + 4096);
      if constexpr (I >= 30 && I < 34) { hp[I - 30] = ws + WS_H + (size_t)((mt * 16 + 4 * q4 + ((c + I - 30) & 3)) * 4 + wv) * HW_B; pins(hp[I - 30]); }
      if constexpr (I == 3) { u32x2 t = *LDSP(u32x2, lds0 + DUMMY_O + 512); tk = 63 - FLAGC - ((u64)(unsigned)__builtin_amdgcn_readfirstlane(t[1]) << 32 | (unsigned)__builtin_amdgcn_readfirstlane(t[0])); }
      if constexpr (I == 8) { rstd[0] = __shfl(rstd[0], q * 16 + n); rstd[1] = __shfl(rstd[1], q * 16 + n); }
      if constexpr (I == 14 || I == 15) { constexpr int b = I - 14; rstd[b] = pinf(__builtin_amdgcn_rsqf(fmaxf(rstd[b], 0.f) + 1e-5f)); sc[b] = pinf(g >= 2 ? rstd[b] : 1.f); }
      if constexpr (I == 24) {
        const u64 mr = __builtin_amdgcn_ballot_w64(sl == 0 && lane < 32);
        asm volatile("s_mov_b64 exec, %3\n\tglobal_store_dword %0, %1, %2 sc1\n\ts_mov_b64 exec, -1" ::"v"(lane * 4), "v"(g == 0 ? rstd[0] : rstd[1]), "s"(ws + WS_RS + (mt * 128 + wv * 32) * 4), "s"(mr));
      }
      if constexpr (I == I_Q - PD - 1) {
#pragma unroll
        for (int i = 0; i < 4; ++i) { rb[i] -= (sl & 1) * 8; pin(rb[i]); }
        rb2[0] = rb[0] + wv * UNIT_B; pin(rb2[0]);
      }
    }
    if constexpr (I >= I_Q && I < I_Q + 28) {
      constexpr int H = I - I_Q, t = H / 4, b = (H / 2) % 2, hh = H % 2;
      if constexpr (A) t0 = t < 4 ? accr(oa[t][b][2 * hh]) : accm(oa[t][b][2 * hh], t == 4 ? sc[b] : rstd[b]);
      else {
        unsigned w = t < 4 ? cvr(t0, oa[t][b][2 * hh + 1]) : cvrm(t0, oa[t][b][2 * hh + 1], t == 4 ? sc[b] : rstd[b]);
        if constexpr (t == 6) w = g == 3 ? (hh ? 0u : 0x3f80u) : w;
        qf[t / 2][b][(t % 2) * 2 + hh] = w;
      }
    }
    if constexpr (I >= I_PQ + 3 && I < I_PQ + 18) { constexpr int K = I - I_PQ - 3, t = K / 3, s = K % 3; pq[t][B] = mfma(wf[K % 4], qf[s][B], s == 0 ? z4 : pq[t][B]); }
    if constexpr (B && I >= I_PQ && I < I_PQ + 15) wf[(I - I_PQ) % 4] = *LDSP(u32x4, lds0 + SPARE + (I - I_PQ) * 1024 + l16);
    if constexpr (B && I == I_VW - 1) { vwa = pinr(vimg + (wv * 32 + n) * 80 + g * 8); vwb = pinr(g >= 2 ? vwa - 16 : vimg + 10240 + lane * 8); }
    if constexpr (A && I >= I_VW && I < I_VW + 6) {
      constexpr int b = (I - I_VW) / 3, K = (I - I_VW) % 3;
      const unsigned va = vwa + b * 1280;
      if constexpr (K == 0) *LDSP(u32x2, vwb + b * 1280) = u32x2{qf[2][b][0], qf[2][b][1]};
      if constexpr (K == 1) *LDSP(u32x2, va + 16) = u32x2{qf[2][b][2], qf[2][b][3]};
      if constexpr (K == 2) *LDSP(u32x2, va + 48) = u32x2{qf[3][b][0], qf[3][b][1]};
    }
    if constexpr (I >= I_SM && I < I_SM + 2 * SMN) {
      constexpr int b = (I - I_SM) / SMN, k = (I - I_SM) % SMN;
      if constexpr (k < 9) {
        if constexpr (A) { p[2 * k] = accr(pq[k / 2][b][(k % 2) * 2]); if constexpr (k == 0) { mx = -3e38f; mn = 3e38f; } }
        else mxn(p[2 * k + 1], mx, mn, p[2 * k], pq[k / 2][b][(k % 2) * 2 + 1]);
      } else if constexpr (k == 9) {
        if constexpr (A) cs = pinf(rstd[b] * 1.44269504f); else { a1 = pinf(-mx * cs); a2 = pinf(mn * cs); }
      } else if constexpr (k < 47) {
        constexpr int j = k - 10, i = j - 1;
        if constexpr (A && j < 36) tt = pinf(j < 18 ? fmaf(p[j], cs, a1) : fmaf(p[j % 18], -cs, a2));
        if constexpr (B && i >= 1 && i != 18) { if constexpr (i < 18) s1 = pinf((i == 1 ? e[0] : s1) + e[i]); else s2 = pinf((i == 19 ? e[18] : s2) + e[i]); }
        if constexpr (B && j < 36) e[j] = pinf(__builtin_amdgcn_exp2f(tt));
        if constexpr (A && j == 36) s1 = pinf(__builtin_amdgcn_rcpf(s1));
      } else if constexpr (k == 47) {
        if constexpr (A) s2 = pinf(__builtin_amdgcn_rcpf(s2)); else { phi[4][b][2] = 0; phi[4][b][3] = 0; }
      } else if constexpr (k < 66) {
        constexpr int w = k - 48;
        if constexpr (A) t0 = pinf(e[2 * w] * (w < 9 ? s1 : s2)); else phi[w / 4][b][w % 4] = cvm(t0, e[2 * w + 1], w < 9 ? s1 : s2);
      }
      if constexpr (B && b == 1 && k >= 50 && k <= 66 && !(k & 1)) { constexpr int w = (k - 50) / 2; *LDSP(u32x2, pwa + 16 * 144 + w * 8) = u32x2{phi[w / 2][1][(w % 2) * 2], phi[w / 2][1][(w % 2) * 2 + 1]}; }
    }
    if constexpr (B && I == I_PW0 - 2) pwa = pinr(g >= 2 ? pimg + (wv * 32 + n) * 144 + (g - 2) * 72 : vimg + 10240 + (lane & 31) * 8);
    if constexpr (B && I >= I_PW0 && I < I_PW0 + 9) { constexpr int w = I - I_PW0; *LDSP(u32x2, pwa + w * 8) = u32x2{phi[w / 2][0][(w % 2) * 2], phi[w / 2][0][(w % 2) * 2 + 1]}; }
    if constexpr (B && I == 40) {
      int hl = wv >> 1, dt = wv & 1; pin(hl); pin(dt);
      kpa = pimg + (4 * g + q) * 144 + hl * 72 + p4 * 8; kva = vimg + (4 * g + q) * 80 + dt * (p4 == 0 ? 32 : 72) + (1 - dt) * (hl * 40 + p4 * 8);
      int d1 = n == 4 ? 18 : n + 16 - 18 * hl, d = dt ? d1 : n + 2 * hl;
      ko = pinr(vimg + 11264 + (hl * 19 + d) * 72 + g * 8); ko = ((dt == 0) | (n == 4) | ((n >> 1) == hl)) ? ko : lds0 + DUMMY_O; pin(ko); pin(kpa); pin(kva);
    }
    if constexpr (B && I >= I_KV + 3 && I < I_KV + 15) { constexpr int K = I - I_KV - 3; kv[K % 3] = mfma(wf[K % 4], qf[(K / 3) % 2][0], K < 3 ? z4 : kv[K % 3]); }
    if constexpr (B && I >= I_KV && I < I_KV + 12) {
      constexpr int K = I - I_KV, ks = K / 3, ft = K % 3;
      if constexpr (ft == 0) qf[ks % 2][0] = cat(trr(kva + ks * 32 * 80), trr(kva + (ks * 32 + 16) * 80));
      wf[K % 4] = cat(trr(kpa + ks * 32 * 144 + ft * 32), trr(kpa + (ks * 32 + 16) * 144 + ft * 32));
    }
    if constexpr (I >= I_KVC && I < I_KVC + 3) { constexpr int K = I - I_KVC; kw[K][B] = cvr(accr(kv[K][2 * B]), kv[K][2 * B + 1]);
      if constexpr (A && K == 2) ko = pinr(g == 0 ? ko : lds0 + DUMMY_O);
      if constexpr (B) *LDSP(u32x2, ko + K * 32) = kw[K];
    }
    if constexpr (B && I == BI(11) - 1) ko = pinr(min(l16, 43 * 16u) + wv * 704);
    if constexpr (B && I == BI(11)) po = *LDSP(u32x4, vimg + 11264 + ko);
    if constexpr (B && I == BI(11) + 1) ko = pinr(ko + mt * KV_B);
    if constexpr ((I >= I_CA && I < I_CA + 72) || (I >= I_CB && I < I_CB + 72)) {
      constexpr int J = I >= I_CB ? I - I_CB : I - I_CA, H = J / 2, t = H / 4, b = (H / 2) % 2, hh = H % 2, G = t + (I >= I_CB ? 9 : 0);
      if constexpr (J % 2 == 0) { if constexpr (A) t1 = accr(oa[t][b][2 * hh]); else t2 = accr(oa[t][b][2 * hh + 1]); }
      else if constexpr (A) asm volatile("v_cvt_pk_bf16_f32 %0, %1, %2" : "=v"(tu) : "v"(t1), "v"(t2));
      else { unsigned r; asm volatile("v_pk_max_i16 %0, %1, 0" : "=v"(r) : "v"(tu)); hf[G / 2][b][(G % 2) * 2 + hh] = r; }
    }
    if constexpr (B && HS >= 0) asm volatile("" ::"v"(hf[HS / 2][HS % 2]));
    if constexpr (B && I == I_KS) asm volatile("" ::"v"(po));
    if constexpr (B && I >= I_OST && I < I_OST + 2) asm volatile("" ::"v"(of[0][I - I_OST]));
    if constexpr (B && I >= I_SUM - 8 && I < I_SUM - 6) {
      constexpr int w = I - I_SUM + 8;
      unsigned v = pinr(((g == q) & (w == (p4 >> 1))) ? (0x3f80u << ((p4 & 1) * 16)) : 0u);
      id[0][w] = v; id[0][2 + w] = 0; id[1][w] = 0; id[1][2 + w] = v;
    }
    if constexpr (I >= I_SUM && I < I_SUM + 16) kv[B] = mfma(id[B], gk[I - I_SUM], I == I_SUM ? z4 : kv[B]);
    if constexpr (I >= I_SUM + 18 && I < I_SUM + 20) { constexpr int w = I - I_SUM - 18; po[2 * w + B] = cvr(accr(kv[w][2 * B]), kv[w][2 * B + 1]); }
    if constexpr (B && I == I_SUM + 20) *LDSP(u32x4, lds0 + SPARE - m8 * 2 * KV_B + ko) = po;
    if constexpr (B && I >= I_SUM - 5 && I < I_SUM - 2) {
      constexpr int t = I - I_SUM + 5;
      int dd = t < 2 ? 9 * (q & 1) + 4 * t + p4 : (p4 == 0 ? 9 * (q & 1) + 8 : 18);
      nda[t] = pinr(lds0 + ((g == (q >> 1) && (t < 2 || p4 < 2)) ? SPARE + (g * 19 + dd) * 72 : ZERO_O));
    }
    if constexpr (I >= I_ND + 3 && I < I_ND + 18) { constexpr int K = I - I_ND - 3, t = (K / 5 + 2) % 3, s = K % 5; nd[t][B] = mfma(wf[K % 4], phi[s][B], s == 0 ? z4 : nd[t][B]); }
    if constexpr (B && I >= I_ND && I < I_ND + 15) {
      constexpr int K = I - I_ND, t = (K / 5 + 2) % 3, s = K % 5;
      u32x2 lo = *LDSP(u32x2, nda[t] + s * 16);
      wf[K % 4] = cat(lo, s == 4 ? lo : *LDSP(u32x2, nda[t] + s * 16 + 8));
    }
    if constexpr (B && (I == I_ND + 19 || I == I_ND + 20)) { constexpr int b = I - I_ND - 19; sc[b] = pinf(__builtin_amdgcn_rcpf((accr(nd[2][b][1]) + 1e-6f) * rstd[b])); }
    if constexpr (I >= I_ND + 21 && I < I_ND + 29) {
      constexpr int b = (I - I_ND - 21) / 4, w = (I - I_ND - 21) % 4;
      if constexpr (A) t0 = accm(nd[w / 2][b][(w % 2) * 2], sc[b]); else of[0][b][w] = cvrm(t0, nd[w / 2][b][(w % 2) * 2 + 1], sc[b]);
    }
    if constexpr (I == I_ND + 29) { if constexpr (A) t0 = accm(nd[2][0][0], sc[0]); else ol[0] = cvrm(t0, nd[2][1][0], sc[1]); }
    if constexpr (B && I == SI(88, 3)) { lf[0] = u32x4{__builtin_amdgcn_perm(ol[1], ol[0], 0x05040100), __builtin_amdgcn_perm(ol[3], ol[2], 0x05040100), 0, 0}; pin(lf[0]); }
    if constexpr (B && I == SI(88, 4)) { lf[1] = u32x4{__builtin_amdgcn_perm(ol[1], ol[0], 0x07060302), __builtin_amdgcn_perm(ol[3], ol[2], 0x07060302), 0, 0}; pin(lf[1]); }
    if constexpr (I >= TAIL + NT && U > 97 - NT && U <= 101 - NT) {
      constexpr int w = U - 98 + NT;
      if constexpr (A) t0 = accr(acc[T - 1][w / 2][(w % 2) * 2]); else po[w] = cvr(t0, acc[T - 1][w / 2][(w % 2) * 2 + 1]);
    }
    if constexpr (B && PS >= 0) st4p<(PS % 4) * 1024>(lk[PS / 4], po, pw);
  };

  setbase(IC(3)); setm0(law + 3 * REG_B);
  sfor<PD>([&](auto I) { rdlo(I); rdhi(I); });
  sfor<NSLOT>([&](auto Ic) {
    constexpr int I = decltype(Ic)::value, U = cu(I), T = ct(I), ST = s2u(I) / 4, GI = g1i(U) % 18;
    const u32x4 a = cat(alo[I % (PD + 1)], ahi[I % (PD + 1)]);
    rdlo(IC(I + PD)); SB();
    if constexpr (I == BI(ST) && ST <= 22) {
      if constexpr (ST < 22) TR(2 * ST); vmw<stage_wait(ST)>();
      if constexpr (ST == 10 || ST == 11 || ST == 17) lbar(); else bar();
      if constexpr (ST < 22) TR(2 * ST + 1);
      if constexpr (ST == 12) flag(WS_FKV + sl * 128 + mt * 8);
      if constexpr (ST == 13) flag(WS_FH + grp + c * 8);
      if constexpr (ST == 17) flag(WS_FB + grp + c * 8);
      if constexpr (ST == 19) flag(WS_FO + grp + c * 8);
    }
    if constexpr (I == I_KK) TR(46);
    if constexpr (I == I_KK) { vmw<between(I_PK, I)>(); spin(pf, n * 8, ws + WS_FKV + sl * 128, tk); }
    if constexpr (I == I_KK) TR(47);
    if constexpr (I == I_KA) TR(48);
    if constexpr (I == I_KA) { vmw<between(I_PA, I)>(); spin(pf, p4 * 8, ws + WS_FH + grp, tk); }
    if constexpr (I == I_KA) TR(49);
    if constexpr (I == I_KB) TR(50);
    if constexpr (I == I_KB) { vmw<between(I_PB, I)>(); spin(pf, p4 * 8, ws + WS_FB + grp, tk); }
    if constexpr (I == I_KB) TR(51);
    if constexpr (I == I_KO) TR(52);
    if constexpr (I == I_KO) { vmw<between(I_PO, I)>(); spin(pf, p4 * 8, ws + WS_FO + grp, tk); }
    if constexpr (I == I_KO) TR(53);
    if constexpr (U >= 65 && U < 88 && T == 0) { vmw<between(islot(U, 1), I)>(); pin(ph[U % (HD + 1)][0]); pin(ph[U % (HD + 1)][1]); }
    if constexpr (I == TAIL + NT - 5) TR(60);
    if constexpr (I == TAIL + NT - 5) { vmw<between(SI(95 - HD, 5), I)>(); sfor<4>([&](auto K) { pin(ph[(92 + K) % (HD + 1)][0]); pin(ph[(92 + K) % (HD + 1)][1]); }); }
    if constexpr (I == TAIL + NT - 5) TR(61);
    if constexpr (I == SI(88, 8)) vmw<between(lastpc(24), I)>();
    if constexpr (I == I_SUM) TR(54);
    if constexpr (I == I_SUM) { vmw<between(I_GAT + 45, I)>(); sfor<16>([&](auto K) { pin(gk[K]); }); }
    if constexpr (I == I_SUM) TR(55);
    if constexpr (I == SI(88, 0)) TR(58);
    if constexpr (I == SI(88, 0)) { vmw<between(I_KO + 12, I)>(); sfor<4>([&](auto K) { pin(of[K][0]); pin(of[K][1]); pin(ol[K]); }); }
    if constexpr (I == SI(88, 0)) TR(59);
    if constexpr (I == I_Q || I == SI(36, 0) || I == I_CB) sfor<9>([&](auto K) { oa[K][0] = acc[K][0]; oa[K][1] = acc[K][1]; });
    if constexpr (wsl(I) && I + PD < NSLOT) __builtin_amdgcn_s_waitcnt(0xC07F | ((I + PD < TAIL ? 2 * PD - 5 : 2 * PD - 7) << 8));
    side(Ic, IC(0)); SB();
    constexpr bool first = (U == 0 || U == 18 || U == 36 || U == 56);
    auto bop = [&](int b) -> u32x4 {
      if constexpr (U < 18) return xf[U][b];
      else if constexpr (U < 56) return xf[GI][b];
      else if constexpr (U < 65) return hf[U - 56][b];
      else if constexpr (U == 96) return lf[b];
      else if constexpr (g2v(U) < 0) return of[U - 88][b];
      else return ph[U % (HD + 1)][b];
    };
    acc[T][0] = mfma(a, bop(0), first ? z4 : acc[T][0]); SB();
    rdhi(IC(I + PD)); SB();
    side(Ic, IC(1)); SB();
    acc[T][1] = mfma(a, bop(1), first ? z4 : acc[T][1]); SB();
  });
  STAMPT(6);
  st4p<0>(lk[2], u32x4{pk2(acc[8][0][0], acc[8][0][1]), pk2(acc[8][0][2], acc[8][0][3]), pk2(acc[8][1][0], acc[8][1][1]), pk2(acc[8][1][2], acc[8][1][3])}, pw);
  asm volatile("s_nop 3");
  { u32x2 v = {64, 0}; asm volatile("s_mov_b64 exec, %3\n\tglobal_atomic_add_x2 %0, %1, %2 sc1\n\ts_mov_b64 exec, -1" ::"v"(0u), "v"(v), "s"(ws + WS_CNT + q4 * 256), "s"(m0)); }
  asm volatile(".Lk1_end:");
#endif
}
__global__ __launch_bounds__(576) void k2(const char* __restrict__ x, const char* __restrict__ ws, char* __restrict__ out) {
  __shared__ __attribute__((aligned(16))) float sm[8 * 580];
  const int tid = threadIdx.x, bid = blockIdx.x, j = (bid >> 3) & 3, blk = (bid >> 5) * 8 + (bid & 7), mt = blk >> 2, wv = blk & 3;
  const int cq = tid >> 2, c = cq / 36, tg = cq - c * 36, nn = tid & 3;
  const u32x4* p = (const u32x4*)(ws + WS_P + (size_t)((mt * 4 + c) * 4 + wv) * 9216) + tg * 16 + 4 * j + nn;
  u32x4 v[4];
#pragma unroll
  for (int i = 0; i < 4; ++i) v[i] = p[(size_t)i * (256 * 576)];
  const int row = tid / 72, ch = tid - row * 72;
  const size_t go = (size_t)(blk * 32 + (row >> 2) * 16 + 4 * j + (row & 3)) * 1152 + ch * 16;
  const u32x4 xv = *(const u32x4*)(x + go);
  const float* rs = (const float*)(ws + WS_RS) + blk * 32 + 4 * j + nn;
  const float r0 = rs[0], r1 = rs[16];
  float a[8];
#pragma unroll
  for (int w = 0; w < 4; ++w) {
    a[2 * w] = 0.f; a[2 * w + 1] = 0.f;
#pragma unroll
    for (int i = 0; i < 4; ++i) { a[2 * w] += __uint_as_float(v[i][w] << 16); a[2 * w + 1] += __uint_as_float(v[i][w] & 0xffff0000u); }
  }
  *(f32x4*)&sm[nn * 580 + cq * 4] = f32x4{a[0], a[1], a[2], a[3]} * r0;
  *(f32x4*)&sm[(4 + nn) * 580 + cq * 4] = f32x4{a[4], a[5], a[6], a[7]} * r1;
  __syncthreads();
  f32x4 s0 = *(const f32x4*)&sm[row * 580 + ch * 8], s1 = *(const f32x4*)&sm[row * 580 + ch * 8 + 4];
  u32x4 o;
  o[0] = pk2(s0[0] + __uint_as_float(xv[0] << 16), s0[1] + __uint_as_float(xv[0] & 0xffff0000u));
  o[1] = pk2(s0[2] + __uint_as_float(xv[1] << 16), s0[3] + __uint_as_float(xv[1] & 0xffff0000u));
  o[2] = pk2(s1[0] + __uint_as_float(xv[2] << 16), s1[1] + __uint_as_float(xv[2] & 0xffff0000u));
  o[3] = pk2(s1[2] + __uint_as_float(xv[3] << 16), s1[3] + __uint_as_float(xv[3] & 0xffff0000u));
  *(u32x4*)(out + go) = o;
#ifdef __HIP_DEVICE_COMPILE__
  asm volatile(".Lk2_end:");
#endif
}

extern "C" void kernel_launch(void* const* d_in, const int* in_sizes, int n_in, void* d_out, int out_size, void* d_ws, size_t ws_size,
                              hipStream_t stream) {
  (void)hipFuncSetAttribute((const void*)k1, hipFuncAttributeMaxDynamicSharedMemorySize, LDS_B);
  k1<<<256, 256, LDS_B, stream>>>((const char*)d_in[0], (const char*)d_in[1], (const char*)d_in[2], (const char*)d_in[3], (const char*)d_in[5],
                                  (const char*)d_in[6], (char*)d_ws);
  k2<<<256, 576, 0, stream>>>((const char*)d_in[0], (const char*)d_ws, (char*)d_out);
}
```
